```python
import torch

def _softmax(x, axis=-1):
    return torch.softmax(x, dim=axis)
def _gelu(x):
    return torch.nn.functional.gelu(x, approximate="tanh")
def _silu(x):
    return torch.nn.functional.silu(x)
def _sigmoid(x):
    return torch.sigmoid(x)
def _layernorm(x, axis=-1, eps=1e-5):
    m = x.mean(dim=axis, keepdim=True)
    v = x.var(dim=axis, keepdim=True, unbiased=False)
    return (x - m) / torch.sqrt(v + eps)
def _scatter_add(tgt, idx, src, axis=0):
    return tgt.clone().index_add_(axis, idx, src)
def _segment_sum(src, seg, n):
    out = torch.zeros((n,) + src.shape[1:], dtype=src.dtype)
    return out.index_add_(0, seg, src)
def _pad(x, pw):
    flat = tuple(v for pair in reversed(pw) for v in pair)
    return torch.nn.functional.pad(x, flat)

kernel_name = 'gather_scatter__molecular_angular_gemnet__graph'

def setup_inputs():

    _w = (lambda a: (lambda t: t.to(torch.bfloat16) if t.is_floating_point() else t)(torch.as_tensor(a)))
    N, deg, De, L = 10000, 16, 4, 3
    E = N * deg
    T = E * deg
    NR = 8
    dist = torch.rand(E).to(float)
    angle = (torch.rand(T) * torch.pi).to(float)
    centers = torch.linspace(0.0, 1.0, NR).to(float)
    ang_freq = torch.arange(1, L + 1).to(float)
    return {
        "m": _w((lambda *s: torch.randn(*s, dtype=torch.bfloat16) * 0.05)(E, De)),
        "idx_in": _w((lambda lo, hi, *s: torch.randint(lo, hi, s))(0, E, T).to(torch.int64)),
        "idx_out": _w((lambda lo, hi, *s: torch.randint(lo, hi, s))(0, E, T).to(torch.int64)),
        "dist": _w(dist),
        "angle": _w(angle),
        "centers": _w(centers),
        "ang_freq": _w(ang_freq),
        "w_rbf": _w((lambda *s: torch.randn(*s, dtype=torch.bfloat16) * 0.2)(NR, De)),
        "w_sbf": _w((lambda *s: torch.randn(*s, dtype=torch.bfloat16) * 0.2)(NR * L, De)),
        "w_down": _w((lambda *s: torch.randn(*s, dtype=torch.bfloat16) * 0.2)(De, De)),
        "w_up": _w((lambda *s: torch.randn(*s, dtype=torch.bfloat16) * 0.2)(De, De)),
    }

def reference(m, idx_in, idx_out, dist, angle, centers, ang_freq, w_rbf, w_sbf, w_down, w_up):
    E, De = m.shape[0], m.shape[1]
    NR = centers.shape[0]
    L = ang_freq.shape[0]
    rbf = torch.exp(-10.0 * (dist[:, None] - centers[None, :]) ** 2)
    cbf = torch.cos(ang_freq[None, :] * angle[:, None])
    rbf_t = rbf[idx_out]
    sbf = torch.reshape(rbf_t[:, :, None] * cbf[:, None, :], (-1, NR * L))
    x = (m[idx_in] @ w_down) * (sbf @ w_sbf)
    agg = _segment_sum(x, idx_out, E)
    return m + (agg @ w_up) * (rbf @ w_rbf)
```

```cpp
#include <hip/hip_runtime.h>
#include <hip/hip_bf16.h>
#include <stdint.h>

static __device__ __forceinline__ float bf2f(uint16_t v) { return __uint_as_float(((uint32_t)v) << 16); }
static __device__ __forceinline__ float rbf16(float x) { return __bfloat162float(__float2bfloat16(x)); }

__global__ void k_trip(const uint16_t* m, const int* idx_in, const int* idx_out, const uint16_t* dist,
                       const uint16_t* angle, const uint16_t* centers, const uint16_t* freq,
                       const uint16_t* w_sbf, const uint16_t* w_down, float* acc, int T) {
    int t = blockIdx.x * blockDim.x + threadIdx.x;
    if (t >= T) return;
    int ei = idx_in[t], eo = idx_out[t];
    float a = bf2f(angle[t]);
    float d = bf2f(dist[eo]);
    float c[3];
    for (int l = 0; l < 3; ++l) c[l] = rbf16(cosf(rbf16(bf2f(freq[l]) * a)));
    float s[4] = {0, 0, 0, 0};
    for (int r = 0; r < 8; ++r) {
        float df = rbf16(d - bf2f(centers[r]));
        float rb = rbf16(expf(rbf16(-10.f * rbf16(df * df))));
        for (int l = 0; l < 3; ++l) {
            float sb = rbf16(rb * c[l]);
            for (int k = 0; k < 4; ++k) s[k] += sb * bf2f(w_sbf[(r * 3 + l) * 4 + k]);
        }
    }
    float mm[4];
    for (int k = 0; k < 4; ++k) mm[k] = bf2f(m[ei * 4 + k]);
    for (int k = 0; k < 4; ++k) {
        float md = 0;
        for (int j = 0; j < 4; ++j) md += mm[j] * bf2f(w_down[j * 4 + k]);
        float x = rbf16(rbf16(md) * rbf16(s[k]));
        atomicAdd(&acc[eo * 4 + k], x);
    }
}

__global__ void k_fin(const uint16_t* m, const uint16_t* dist, const uint16_t* centers, const uint16_t* w_rbf,
                      const uint16_t* w_up, const float* acc, __hip_bfloat16* out, int E) {
    int e = blockIdx.x * blockDim.x + threadIdx.x;
    if (e >= E) return;
    float d = bf2f(dist[e]);
    float g[4] = {0, 0, 0, 0};
    for (int r = 0; r < 8; ++r) {
        float df = rbf16(d - bf2f(centers[r]));
        float rb = rbf16(expf(rbf16(-10.f * rbf16(df * df))));
        for (int k = 0; k < 4; ++k) g[k] += rb * bf2f(w_rbf[r * 4 + k]);
    }
    float ag[4];
    for (int k = 0; k < 4; ++k) ag[k] = rbf16(acc[e * 4 + k]);
    for (int k = 0; k < 4; ++k) {
        float u = 0;
        for (int j = 0; j < 4; ++j) u += ag[j] * bf2f(w_up[j * 4 + k]);
        float y = rbf16(rbf16(u) * rbf16(g[k]));
        out[e * 4 + k] = __float2bfloat16(bf2f(m[e * 4 + k]) + y);
    }
}

extern "C" void kernel_launch(void* const* d_in, const int* in_sizes, int n_in,
                              void* d_out, int out_size, void* d_ws, size_t ws_size,
                              hipStream_t stream) {
    const uint16_t* m = (const uint16_t*)d_in[0];
    const int* idx_in = (const int*)d_in[1];
    const int* idx_out = (const int*)d_in[2];
    const uint16_t* dist = (const uint16_t*)d_in[3];
    const uint16_t* angle = (const uint16_t*)d_in[4];
    const uint16_t* centers = (const uint16_t*)d_in[5];
    const uint16_t* freq = (const uint16_t*)d_in[6];
    const uint16_t* w_rbf = (const uint16_t*)d_in[7];
    const uint16_t* w_sbf = (const uint16_t*)d_in[8];
    const uint16_t* w_down = (const uint16_t*)d_in[9];
    const uint16_t* w_up = (const uint16_t*)d_in[10];
    int T = in_sizes[1];
    int E = in_sizes[3];
    float* acc = (float*)d_ws;
    hipMemsetAsync(acc, 0, (size_t)E * 4 * sizeof(float), stream);
    k_trip<<<(T + 255) / 256, 256, 0, stream>>>(m, idx_in, idx_out, dist, angle, centers, freq, w_sbf, w_down, acc, T);
    k_fin<<<(E + 255) / 256, 256, 0, stream>>>(m, dist, centers, w_rbf, w_up, acc, (__hip_bfloat16*)d_out, E);
}
```

```cpp
#include <hip/hip_runtime.h>
#include <hip/hip_bf16.h>
#include <stdint.h>

typedef unsigned int u32;
typedef unsigned short u16;
typedef u32 u32x2 __attribute__((ext_vector_type(2)));
typedef u32 u32x4 __attribute__((ext_vector_type(4)));
typedef float f32x4 __attribute__((ext_vector_type(4)));
typedef float f32x2 __attribute__((ext_vector_type(2)));
typedef __attribute__((address_space(3))) float lds_f32;

static __device__ __forceinline__ float bf2f(u16 v) { return __uint_as_float(((u32)v) << 16); }
static __device__ __forceinline__ float rbf16(float x) { return __bfloat162float(__float2bfloat16(x)); }
static __device__ __forceinline__ float lo16f(u32 v) { return __uint_as_float(v << 16); }
static __device__ __forceinline__ float hi16f(u32 v) { return __uint_as_float(v & 0xFFFF0000u); }
static __device__ __forceinline__ u32 pk_bf16(float a, float b) {
    typedef __bf16 bf16x2 __attribute__((ext_vector_type(2)));
    bf16x2 r = __builtin_convertvector((f32x2){a, b}, bf16x2);
    return __builtin_bit_cast(u32, r);
}

namespace fast {
constexpr int kE = 160000, kT = 2560000;
constexpr int NB = 256, EB = 625, NC = 256, S = 10000;
constexpr int CAP = 64, STRIDE = 528;
constexpr u32 MAGIC = 6871948u;
constexpr size_t REC_BYTES = (size_t)NB * NC * CAP * 8;
constexpr size_t OFF_MD = REC_BYTES;
constexpr size_t OFF_OVC = OFF_MD + (size_t)kE * 8;
constexpr size_t OFF_OVR = OFF_OVC + 4096;
constexpr size_t WS_NEED = OFF_OVR + (size_t)NC * S * 8;

constexpr int P1_LDS = NB * STRIDE + 1024 + 16;

struct Rec { u32 w0, w1, b, addr; };

static __device__ __forceinline__ void bin1(u32 out, u32 in, u32 angw, u32* cnt, Rec& r) {
    u32 b = __umulhi(out & 0xFFFFFFu, MAGIC);
    u32 l16 = (u32)(__mul24((int)b, -10000) + (int)(out << 4));
    r.w0 = in << 3;
    r.w1 = angw | l16;
    r.b = b;
    r.addr = __hip_atomic_fetch_add(&cnt[b], 8u, __ATOMIC_RELAXED, __HIP_MEMORY_SCOPE_WORKGROUP);
}

static __device__ __forceinline__ void put1(const Rec& r, char* smem, u32* ovc, u32x2* ovr) {
    u32 lim = r.b * STRIDE + CAP * 8;
    if (__builtin_expect(r.addr < lim, 1)) {
        *(u32x2*)(smem + r.addr) = (u32x2){r.w0, r.w1};
    } else {
        u32 k = __hip_atomic_fetch_add(ovc, 1u, __ATOMIC_RELAXED, __HIP_MEMORY_SCOPE_WORKGROUP);
        ovr[k] = (u32x2){r.w0 | (r.b << 21), r.w1};
    }
}

__global__ void __launch_bounds__(1024)
k_bin(const u32* __restrict__ idx_in, const u32* __restrict__ idx_out, const u16* __restrict__ angle,
      const u16* __restrict__ m, const u16* __restrict__ w_down, char* __restrict__ ws) {
    __shared__ __attribute__((aligned(16))) char smem[P1_LDS];
    u32* cnt = (u32*)(smem + NB * STRIDE);
    u32* ovc = cnt + 256;
    const int c = blockIdx.x, t = threadIdx.x;
    const u32* po = idx_out + c * S;
    const u32* pi = idx_in + c * S;
    const u16* pa = angle + c * S;
    u32x4 o0 = *(const u32x4*)(po + 4 * t);
    u32x4 i0 = *(const u32x4*)(pi + 4 * t);
    u32x2 a0 = *(const u32x2*)(pa + 4 * t);
    u32x4 o1 = *(const u32x4*)(po + 4096 + 4 * t);
    u32x4 i1 = *(const u32x4*)(pi + 4096 + 4 * t);
    u32x2 a1 = *(const u32x2*)(pa + 4096 + 4 * t);
    const bool has2 = t < 904;
    const int t2 = has2 ? t : 0;
    u32x2 o2 = *(const u32x2*)(po + 8192 + 2 * t2);
    u32x2 i2 = *(const u32x2*)(pi + 8192 + 2 * t2);
    u32 a2 = *(const u32*)(pa + 8192 + 2 * t2);
    u32x2 mrow = (u32x2){0, 0};
    if (t < EB) mrow = *(const u32x2*)(m + (size_t)(c * EB + t) * 4);
    if (t < 256) cnt[t] = t * STRIDE;
    if (t == 256) *ovc = 0;
    __syncthreads();
    u32x2* ovr = (u32x2*)(ws + OFF_OVR) + (size_t)c * S;
    {
        Rec r0, r1, r2, r3;
        bin1(o0.x, i0.x, a0.x << 16, cnt, r0);
        bin1(o0.y, i0.y, a0.x & 0xFFFF0000u, cnt, r1);
        bin1(o0.z, i0.z, a0.y << 16, cnt, r2);
        bin1(o0.w, i0.w, a0.y & 0xFFFF0000u, cnt, r3);
        put1(r0, smem, ovc, ovr); put1(r1, smem, ovc, ovr); put1(r2, smem, ovc, ovr); put1(r3, smem, ovc, ovr);
    }
    {
        Rec r0, r1, r2, r3;
        bin1(o1.x, i1.x, a1.x << 16, cnt, r0);
        bin1(o1.y, i1.y, a1.x & 0xFFFF0000u, cnt, r1);
        bin1(o1.z, i1.z, a1.y << 16, cnt, r2);
        bin1(o1.w, i1.w, a1.y & 0xFFFF0000u, cnt, r3);
        put1(r0, smem, ovc, ovr); put1(r1, smem, ovc, ovr); put1(r2, smem, ovc, ovr); put1(r3, smem, ovc, ovr);
    }
    if (has2) {
        Rec r0, r1;
        bin1(o2.x, i2.x, a2 << 16, cnt, r0);
        bin1(o2.y, i2.y, a2 & 0xFFFF0000u, cnt, r1);
        put1(r0, smem, ovc, ovr); put1(r1, smem, ovc, ovr);
    }
    if (t < EB) {
        float mm[4] = {lo16f(mrow.x), hi16f(mrow.x), lo16f(mrow.y), hi16f(mrow.y)};
        float md[4];
#pragma unroll
        for (int k = 0; k < 4; ++k) {
            float s = 0.f;
#pragma unroll
            for (int j = 0; j < 4; ++j) s += mm[j] * bf2f(w_down[j * 4 + k]);
            md[k] = s;
        }
        *((u32x2*)(ws + OFF_MD) + c * EB + t) = (u32x2){pk_bf16(md[0], md[1]), pk_bf16(md[2], md[3])};
    }
    __syncthreads();
    const int w = t >> 6, lane = t & 63, j = lane & 31;
    u32x4 v[8];
    u32 len[8];
#pragma unroll
    for (int i = 0; i < 8; ++i) {
        int b = w * 16 + i * 2 + (lane >> 5);
        len[i] = min((cnt[b] - (u32)(b * STRIDE)) >> 3, (u32)CAP);
        v[i] = *(const u32x4*)(smem + b * STRIDE + j * 16);
    }
#pragma unroll
    for (int i = 0; i < 8; ++i) {
        int b = w * 16 + i * 2 + (lane >> 5);
        if (j == 0) v[i].x = (v[i].x & 0x1FFFFFu) | (len[i] << 21);
        if ((u32)((2 * j) & ~7) < len[i] || j == 0)
            *(u32x4*)(ws + ((size_t)(b * NC + c) * CAP * 8) + j * 16) = v[i];
    }
    if (t == 0) ((u32*)(ws + OFF_OVC))[c] = *ovc;
}

constexpr int A_OFF = 0;
constexpr int A_STR = 640 * 16;
constexpr int ACC_OFF = 3 * A_STR;
constexpr int ACC_STR = 640 * 8;
constexpr int P2_LDS = ACC_OFF + 4 * ACC_STR;

static __device__ __forceinline__ float cos_rev(float x) {
    return __builtin_amdgcn_cosf(__builtin_amdgcn_fractf(x * 0.15915494309189535f));
}

static __device__ __forceinline__ void accum(u32 w1, u32x2 md, float f0, float f1, float f2, char* smem) {
    float a = hi16f(w1);
    u32 p01 = pk_bf16(f0 * a, f1 * a);
    u32 p2 = pk_bf16(f2 * a, f2 * a);
    float c0 = cos_rev(lo16f(p01)), c1 = cos_rev(hi16f(p01)), c2 = cos_rev(hi16f(p2));
    u32 l16 = w1 & 0xFFFFu;
    f32x4 A0 = *(const f32x4*)(smem + A_OFF + l16);
    f32x4 A1 = *(const f32x4*)(smem + A_OFF + A_STR + l16);
    f32x4 A2 = *(const f32x4*)(smem + A_OFF + 2 * A_STR + l16);
    f32x4 s = A0 * c0 + A1 * c1 + A2 * c2;
    f32x4 x = (f32x4){lo16f(md.x), hi16f(md.x), lo16f(md.y), hi16f(md.y)} * s;
    double* acc = (double*)(smem + ACC_OFF + (l16 >> 1));
    __hip_atomic_fetch_add(acc, (double)x.x, __ATOMIC_RELAXED, __HIP_MEMORY_SCOPE_WORKGROUP);
    __hip_atomic_fetch_add(acc + 640, (double)x.y, __ATOMIC_RELAXED, __HIP_MEMORY_SCOPE_WORKGROUP);
    __hip_atomic_fetch_add(acc + 1280, (double)x.z, __ATOMIC_RELAXED, __HIP_MEMORY_SCOPE_WORKGROUP);
    __hip_atomic_fetch_add(acc + 1920, (double)x.w, __ATOMIC_RELAXED, __HIP_MEMORY_SCOPE_WORKGROUP);
}

__global__ void __launch_bounds__(1024)
k_acc(const u16* __restrict__ m, const u16* __restrict__ dist, const u16* __restrict__ centers,
      const u16* __restrict__ freq, const u16* __restrict__ w_rbf, const u16* __restrict__ w_sbf,
      const u16* __restrict__ w_up, const char* __restrict__ ws, u16* __restrict__ out) {
    __shared__ __attribute__((aligned(16))) char smem[P2_LDS];
    const int b = blockIdx.x, t = threadIdx.x;
    const int w = t >> 6, lane = t & 63, j = lane & 31;
    const char* seg = ws + (size_t)(b * NC + w * 16 + (lane >> 5)) * (CAP * 8) + j * 16;
    u32x4 r[8];
#pragma unroll
    for (int i = 0; i < 8; ++i) r[i] = *(const u32x4*)(seg + i * 2 * CAP * 8);
    u32 ovn = 0;
    if (t < NC) ovn = ((const u32*)(ws + OFF_OVC))[t];
    const int e = b * EB + t;
    u32x2 mrow = (u32x2){0, 0};
    float g[4] = {0.f, 0.f, 0.f, 0.f};
    if (t < EB) {
        mrow = *(const u32x2*)(m + (size_t)e * 4);
        float d = bf2f(dist[e]);
        f32x4 A0 = {0, 0, 0, 0}, A1 = {0, 0, 0, 0}, A2 = {0, 0, 0, 0};
#pragma unroll
        for (int q = 0; q < 8; ++q) {
            float df = rbf16(d - bf2f(centers[q]));
            float rb = rbf16(__expf(rbf16(-10.f * rbf16(df * df))));
#pragma unroll
            for (int k = 0; k < 4; ++k) {
                A0[k] += rb * bf2f(w_sbf[(q * 3 + 0) * 4 + k]);
                A1[k] += rb * bf2f(w_sbf[(q * 3 + 1) * 4 + k]);
                A2[k] += rb * bf2f(w_sbf[(q * 3 + 2) * 4 + k]);
                g[k] += rb * bf2f(w_rbf[q * 4 + k]);
            }
        }
        *(f32x4*)(smem + A_OFF + t * 16) = A0;
        *(f32x4*)(smem + A_OFF + A_STR + t * 16) = A1;
        *(f32x4*)(smem + A_OFF + 2 * A_STR + t * 16) = A2;
    }
    if (t < 640) { *(f32x4*)(smem + ACC_OFF + t * 32) = (f32x4){0, 0, 0, 0}; *(f32x4*)(smem + ACC_OFF + t * 32 + 16) = (f32x4){0, 0, 0, 0}; }
    const float f0 = bf2f(freq[0]), f1 = bf2f(freq[1]), f2 = bf2f(freq[2]);
    __amdgpu_buffer_rsrc_t rs = __builtin_amdgcn_make_buffer_rsrc((void*)(ws + OFF_MD), 0, kE * 8, 0x00020000);
    u32x2 ga[8], gb[8];
    u32 vm = 0;
#pragma unroll
    for (int i = 0; i < 8; ++i) {
        u32 la = __builtin_amdgcn_readlane(r[i].x, 0) >> 21, lb = __builtin_amdgcn_readlane(r[i].x, 32) >> 21;
        u32 len = lane < 32 ? la : lb;
        bool va = (u32)(2 * j) < len, vb = (u32)(2 * j + 1) < len;
        ga[i] = __builtin_amdgcn_raw_buffer_load_b64(rs, va ? (r[i].x & 0x1FFFFFu) : 0xFFFFFFF0u, 0, 0);
        gb[i] = __builtin_amdgcn_raw_buffer_load_b64(rs, vb ? r[i].z : 0xFFFFFFF0u, 0, 0);
        vm |= (va ? 1u : 0u) << (2 * i) | (vb ? 2u : 0u) << (2 * i);
    }
    __syncthreads();
#pragma unroll
    for (int i = 0; i < 8; ++i) {
        if (vm & (1u << (2 * i))) accum(r[i].y, ga[i], f0, f1, f2, smem);
        if (vm & (2u << (2 * i))) accum(r[i].w, gb[i], f0, f1, f2, smem);
    }
    if (t < NC) {
        const u32x2* ovr = (const u32x2*)(ws + OFF_OVR) + (size_t)t * S;
        for (u32 k = 0; k < ovn; ++k) {
            u32x2 q = ovr[k];
            if ((int)(q.x >> 21) == b) {
                u32x2 md = *(const u32x2*)(ws + OFF_MD + (q.x & 0x1FFFFFu));
                accum(q.y, md, f0, f1, f2, smem);
            }
        }
    }
    __syncthreads();
    if (t < EB) {
        const double* acc = (const double*)(smem + ACC_OFF) + t;
        float ag[4] = {rbf16((float)acc[0]), rbf16((float)acc[640]), rbf16((float)acc[1280]), rbf16((float)acc[1920])};
        float mm[4] = {lo16f(mrow.x), hi16f(mrow.x), lo16f(mrow.y), hi16f(mrow.y)};
        float o[4];
#pragma unroll
        for (int k = 0; k < 4; ++k) {
            float u = 0.f;
#pragma unroll
            for (int q = 0; q < 4; ++q) u += ag[q] * bf2f(w_up[q * 4 + k]);
            o[k] = mm[k] + rbf16(rbf16(u) * rbf16(g[k]));
        }
        *(u32x2*)(out + (size_t)e * 4) = (u32x2){pk_bf16(o[0], o[1]), pk_bf16(o[2], o[3])};
    }
}
}

extern "C" void kernel_launch(void* const* d_in, const int* in_sizes, int n_in,
                              void* d_out, int out_size, void* d_ws, size_t ws_size,
                              hipStream_t stream) {
    const u16* m = (const u16*)d_in[0];
    const u32* idx_in = (const u32*)d_in[1];
    const u32* idx_out = (const u32*)d_in[2];
    const u16* dist = (const u16*)d_in[3];
    const u16* angle = (const u16*)d_in[4];
    const u16* centers = (const u16*)d_in[5];
    const u16* freq = (const u16*)d_in[6];
    const u16* w_rbf = (const u16*)d_in[7];
    const u16* w_sbf = (const u16*)d_in[8];
    const u16* w_down = (const u16*)d_in[9];
    const u16* w_up = (const u16*)d_in[10];
    fast::k_bin<<<fast::NC, 1024, 0, stream>>>(idx_in, idx_out, angle, m, w_down, (char*)d_ws);
    fast::k_acc<<<fast::NB, 1024, 0, stream>>>(m, dist, centers, freq, w_rbf, w_sbf, w_up, (const char*)d_ws,
                                               (u16*)d_out);
}
```

```cpp
#include <hip/hip_runtime.h>
#include <hip/hip_bf16.h>
#include <stdint.h>

typedef unsigned int u32;
typedef unsigned short u16;
typedef u32 u32x2 __attribute__((ext_vector_type(2)));
typedef u32 u32x4 __attribute__((ext_vector_type(4)));
typedef float f32x4 __attribute__((ext_vector_type(4)));
typedef float f32x2 __attribute__((ext_vector_type(2)));
typedef __attribute__((address_space(3))) float lds_f32;

static __device__ __forceinline__ float bf2f(u16 v) { return __uint_as_float(((u32)v) << 16); }
static __device__ __forceinline__ float rbf16(float x) { return __bfloat162float(__float2bfloat16(x)); }
static __device__ __forceinline__ float lo16f(u32 v) { return __uint_as_float(v << 16); }
static __device__ __forceinline__ float hi16f(u32 v) { return __uint_as_float(v & 0xFFFF0000u); }
static __device__ __forceinline__ u32 pk_bf16(float a, float b) {
    typedef __bf16 bf16x2 __attribute__((ext_vector_type(2)));
    bf16x2 r = __builtin_convertvector((f32x2){a, b}, bf16x2);
    return __builtin_bit_cast(u32, r);
}

namespace fast {
constexpr int kE = 160000, kT = 2560000;
constexpr int NB = 256, EB = 625, NC = 256, S = 10000;
constexpr int CAP = 64, STRIDE = 528;
constexpr u32 MAGIC = 6871948u;
constexpr size_t REC_BYTES = (size_t)NB * NC * CAP * 8;
constexpr size_t OFF_MD = REC_BYTES;
constexpr size_t OFF_OVC = OFF_MD + (size_t)kE * 8;
constexpr size_t OFF_OVR = OFF_OVC + 4096;
constexpr size_t WS_NEED = OFF_OVR + (size_t)NC * S * 8;

constexpr int P1_LDS = NB * STRIDE + 1024 + 16;

struct Rec { u32 w0, w1, b, addr; };

static __device__ __forceinline__ void bin1(u32 out, u32 in, u32 angw, u32* cnt, Rec& r) {
    u32 b = __umulhi(out & 0xFFFFFFu, MAGIC);
    u32 l16 = (u32)(__mul24((int)b, -10000) + (int)(out << 4));
    r.w0 = in << 3;
    r.w1 = angw | l16;
    r.b = b;
    r.addr = __hip_atomic_fetch_add(&cnt[b], 8u, __ATOMIC_RELAXED, __HIP_MEMORY_SCOPE_WORKGROUP);
}

static __device__ __forceinline__ void put1(const Rec& r, char* smem, u32* ovc, u32x2* ovr) {
    u32 lim = r.b * STRIDE + CAP * 8;
    if (__builtin_expect(r.addr < lim, 1)) {
        *(u32x2*)(smem + r.addr) = (u32x2){r.w0, r.w1};
    } else {
        u32 k = __hip_atomic_fetch_add(ovc, 1u, __ATOMIC_RELAXED, __HIP_MEMORY_SCOPE_WORKGROUP);
        ovr[k] = (u32x2){r.w0 | (r.b << 21), r.w1};
    }
}

__global__ void __launch_bounds__(1024)
k_bin(const u32* __restrict__ idx_in, const u32* __restrict__ idx_out, const u16* __restrict__ angle,
      const u16* __restrict__ m, const u16* __restrict__ w_down, char* __restrict__ ws) {
    __shared__ __attribute__((aligned(16))) char smem[P1_LDS];
    u32* cnt = (u32*)(smem + NB * STRIDE);
    u32* ovc = cnt + 256;
    const int c = blockIdx.x, t = threadIdx.x;
    const u32* po = idx_out + c * S;
    const u32* pi = idx_in + c * S;
    const u16* pa = angle + c * S;
    u32x4 o0 = *(const u32x4*)(po + 4 * t);
    u32x4 i0 = *(const u32x4*)(pi + 4 * t);
    u32x2 a0 = *(const u32x2*)(pa + 4 * t);
    u32x4 o1 = *(const u32x4*)(po + 4096 + 4 * t);
    u32x4 i1 = *(const u32x4*)(pi + 4096 + 4 * t);
    u32x2 a1 = *(const u32x2*)(pa + 4096 + 4 * t);
    const bool has2 = t < 904;
    const int t2 = has2 ? t : 0;
    u32x2 o2 = *(const u32x2*)(po + 8192 + 2 * t2);
    u32x2 i2 = *(const u32x2*)(pi + 8192 + 2 * t2);
    u32 a2 = *(const u32*)(pa + 8192 + 2 * t2);
    u32x2 mrow = (u32x2){0, 0};
    if (t < EB) mrow = *(const u32x2*)(m + (size_t)(c * EB + t) * 4);
    if (t < 256) cnt[t] = t * STRIDE;
    if (t == 256) *ovc = 0;
    __syncthreads();
    u32x2* ovr = (u32x2*)(ws + OFF_OVR) + (size_t)c * S;
    {
        Rec r0, r1, r2, r3;
        bin1(o0.x, i0.x, a0.x << 16, cnt, r0);
        bin1(o0.y, i0.y, a0.x & 0xFFFF0000u, cnt, r1);
        bin1(o0.z, i0.z, a0.y << 16, cnt, r2);
        bin1(o0.w, i0.w, a0.y & 0xFFFF0000u, cnt, r3);
        put1(r0, smem, ovc, ovr); put1(r1, smem, ovc, ovr); put1(r2, smem, ovc, ovr); put1(r3, smem, ovc, ovr);
    }
    {
        Rec r0, r1, r2, r3;
        bin1(o1.x, i1.x, a1.x << 16, cnt, r0);
        bin1(o1.y, i1.y, a1.x & 0xFFFF0000u, cnt, r1);
        bin1(o1.z, i1.z, a1.y << 16, cnt, r2);
        bin1(o1.w, i1.w, a1.y & 0xFFFF0000u, cnt, r3);
        put1(r0, smem, ovc, ovr); put1(r1, smem, ovc, ovr); put1(r2, smem, ovc, ovr); put1(r3, smem, ovc, ovr);
    }
    if (has2) {
        Rec r0, r1;
        bin1(o2.x, i2.x, a2 << 16, cnt, r0);
        bin1(o2.y, i2.y, a2 & 0xFFFF0000u, cnt, r1);
        put1(r0, smem, ovc, ovr); put1(r1, smem, ovc, ovr);
    }
    if (t < EB) {
        float mm[4] = {lo16f(mrow.x), hi16f(mrow.x), lo16f(mrow.y), hi16f(mrow.y)};
        float md[4];
#pragma unroll
        for (int k = 0; k < 4; ++k) {
            float s = 0.f;
#pragma unroll
            for (int j = 0; j < 4; ++j) s += mm[j] * bf2f(w_down[j * 4 + k]);
            md[k] = s;
        }
        *((u32x2*)(ws + OFF_MD) + c * EB + t) = (u32x2){pk_bf16(md[0], md[1]), pk_bf16(md[2], md[3])};
    }
    __syncthreads();
    const int w = t >> 6, lane = t & 63, j = lane & 31;
    u32x4 v[8];
    u32 len[8];
#pragma unroll
    for (int i = 0; i < 8; ++i) {
        int b = w * 16 + i * 2 + (lane >> 5);
        len[i] = min((cnt[b] - (u32)(b * STRIDE)) >> 3, (u32)CAP);
        v[i] = *(const u32x4*)(smem + b * STRIDE + j * 16);
    }
#pragma unroll
    for (int i = 0; i < 8; ++i) {
        int b = w * 16 + i * 2 + (lane >> 5);
        if (j == 0) v[i].x = (v[i].x & 0x1FFFFFu) | (len[i] << 21);
        if ((u32)((2 * j) & ~7) < len[i] || j == 0)
            *(u32x4*)(ws + ((size_t)(b * NC + c) * CAP * 8) + j * 16) = v[i];
    }
    if (t == 0) ((u32*)(ws + OFF_OVC))[c] = *ovc;
}

constexpr int A_OFF = 0;
constexpr int A_STR = 640 * 16;
constexpr int ACC_OFF = 3 * A_STR;
constexpr int ACC_STR = 640 * 8;
constexpr int P2_LDS = ACC_OFF + 4 * ACC_STR;

static __device__ __forceinline__ float cos_rev(float x) {
    return __builtin_amdgcn_cosf(__builtin_amdgcn_fractf(x * 0.15915494309189535f));
}

static __device__ __forceinline__ void accum(u32 w1, u32x2 md, float f0, float f1, float f2, char* smem) {
    float a = hi16f(w1);
    u32 p01 = pk_bf16(f0 * a, f1 * a);
    u32 p2 = pk_bf16(f2 * a, f2 * a);
    float c0 = cos_rev(lo16f(p01)), c1 = cos_rev(hi16f(p01)), c2 = cos_rev(hi16f(p2));
    u32 l16 = w1 & 0xFFFFu;
    f32x4 A0 = *(const f32x4*)(smem + A_OFF + l16);
    f32x4 A1 = *(const f32x4*)(smem + A_OFF + A_STR + l16);
    f32x4 A2 = *(const f32x4*)(smem + A_OFF + 2 * A_STR + l16);
    f32x4 s = A0 * c0 + A1 * c1 + A2 * c2;
    f32x4 x = (f32x4){lo16f(md.x), hi16f(md.x), lo16f(md.y), hi16f(md.y)} * s;
    double* acc = (double*)(smem + ACC_OFF + (l16 >> 1));
    __hip_atomic_fetch_add(acc, (double)x.x, __ATOMIC_RELAXED, __HIP_MEMORY_SCOPE_WORKGROUP);
    __hip_atomic_fetch_add(acc + 640, (double)x.y, __ATOMIC_RELAXED, __HIP_MEMORY_SCOPE_WORKGROUP);
    __hip_atomic_fetch_add(acc + 1280, (double)x.z, __ATOMIC_RELAXED, __HIP_MEMORY_SCOPE_WORKGROUP);
    __hip_atomic_fetch_add(acc + 1920, (double)x.w, __ATOMIC_RELAXED, __HIP_MEMORY_SCOPE_WORKGROUP);
}

__global__ void __launch_bounds__(1024)
k_acc(const u16* __restrict__ m, const u16* __restrict__ dist, const u16* __restrict__ centers,
      const u16* __restrict__ freq, const u16* __restrict__ w_rbf, const u16* __restrict__ w_sbf,
      const u16* __restrict__ w_up, const char* __restrict__ ws, u16* __restrict__ out) {
    __shared__ __attribute__((aligned(16))) char smem[P2_LDS];
    const int b = blockIdx.x, t = threadIdx.x;
    const int w = t >> 6, lane = t & 63, j = lane & 31;
    const char* seg = ws + (size_t)(b * NC + w * 16 + (lane >> 5)) * (CAP * 8) + j * 16;
    u32x4 r[8];
#pragma unroll
    for (int i = 0; i < 8; ++i) r[i] = *(const u32x4*)(seg + i * 2 * CAP * 8);
    u32 ovn = 0;
    if (t < NC) ovn = ((const u32*)(ws + OFF_OVC))[t];
    const int e = b * EB + t;
    u32x2 mrow = (u32x2){0, 0};
    float g[4] = {0.f, 0.f, 0.f, 0.f};
    if (t < EB) {
        mrow = *(const u32x2*)(m + (size_t)e * 4);
        float d = bf2f(dist[e]);
        f32x4 A0 = {0, 0, 0, 0}, A1 = {0, 0, 0, 0}, A2 = {0, 0, 0, 0};
#pragma unroll
        for (int q = 0; q < 8; ++q) {
            float df = rbf16(d - bf2f(centers[q]));
            float rb = rbf16(__expf(rbf16(-10.f * rbf16(df * df))));
#pragma unroll
            for (int k = 0; k < 4; ++k) {
                A0[k] += rb * bf2f(w_sbf[(q * 3 + 0) * 4 + k]);
                A1[k] += rb * bf2f(w_sbf[(q * 3 + 1) * 4 + k]);
                A2[k] += rb * bf2f(w_sbf[(q * 3 + 2) * 4 + k]);
                g[k] += rb * bf2f(w_rbf[q * 4 + k]);
            }
        }
        *(f32x4*)(smem + A_OFF + t * 16) = A0;
        *(f32x4*)(smem + A_OFF + A_STR + t * 16) = A1;
        *(f32x4*)(smem + A_OFF + 2 * A_STR + t * 16) = A2;
    }
    if (t < 640) { *(f32x4*)(smem + ACC_OFF + t * 32) = (f32x4){0, 0, 0, 0}; *(f32x4*)(smem + ACC_OFF + t * 32 + 16) = (f32x4){0, 0, 0, 0}; }
    const float f0 = bf2f(freq[0]), f1 = bf2f(freq[1]), f2 = bf2f(freq[2]);
    __amdgpu_buffer_rsrc_t rs = __builtin_amdgcn_make_buffer_rsrc((void*)(ws + OFF_MD), 0, kE * 8, 0x00020000);
    u32x2 ga[8], gb[8];
    u32 vm = 0;
#pragma unroll
    for (int i = 0; i < 8; ++i) {
        u32 la = __builtin_amdgcn_readlane(r[i].x, 0) >> 21, lb = __builtin_amdgcn_readlane(r[i].x, 32) >> 21;
        u32 len = lane < 32 ? la : lb;
        bool va = (u32)(2 * j) < len, vb = (u32)(2 * j + 1) < len;
        ga[i] = __builtin_amdgcn_raw_buffer_load_b64(rs, va ? (r[i].x & 0x1FFFFFu) : 0xFFFFFFF0u, 0, 0);
        gb[i] = __builtin_amdgcn_raw_buffer_load_b64(rs, vb ? r[i].z : 0xFFFFFFF0u, 0, 0);
        vm |= (va ? 1u : 0u) << (2 * i) | (vb ? 2u : 0u) << (2 * i);
    }
    __syncthreads();
#pragma unroll
    for (int i = 0; i < 8; ++i) {
        if (vm & (1u << (2 * i))) accum(r[i].y, ga[i], f0, f1, f2, smem);
        if (vm & (2u << (2 * i))) accum(r[i].w, gb[i], f0, f1, f2, smem);
    }
    if (t < NC) {
        const u32x2* ovr = (const u32x2*)(ws + OFF_OVR) + (size_t)t * S;
        for (u32 k = 0; k < ovn; ++k) {
            u32x2 q = ovr[k];
            if ((int)(q.x >> 21) == b) {
                u32x2 md = *(const u32x2*)(ws + OFF_MD + (q.x & 0x1FFFFFu));
                accum(q.y, md, f0, f1, f2, smem);
            }
        }
    }
    __syncthreads();
    if (t < EB) {
        const double* acc = (const double*)(smem + ACC_OFF) + t;
        float ag[4] = {rbf16((float)acc[0]), rbf16((float)acc[640]), rbf16((float)acc[1280]), rbf16((float)acc[1920])};
        float mm[4] = {lo16f(mrow.x), hi16f(mrow.x), lo16f(mrow.y), hi16f(mrow.y)};
        float o[4];
#pragma unroll
        for (int k = 0; k < 4; ++k) {
            float u = 0.f;
#pragma unroll
            for (int q = 0; q < 4; ++q) u += ag[q] * bf2f(w_up[q * 4 + k]);
            o[k] = mm[k] + rbf16(rbf16(u) * rbf16(g[k]));
        }
        *(u32x2*)(out + (size_t)e * 4) = (u32x2){pk_bf16(o[0], o[1]), pk_bf16(o[2], o[3])};
    }
}
}

namespace generic {
__global__ void k_trip(const u16* m, const int* idx_in, const int* idx_out, const u16* dist,
                       const u16* angle, const u16* centers, const u16* freq,
                       const u16* w_sbf, const u16* w_down, float* acc, int T) {
    int t = blockIdx.x * blockDim.x + threadIdx.x;
    if (t >= T) return;
    int ei = idx_in[t], eo = idx_out[t];
    float a = bf2f(angle[t]);
    float d = bf2f(dist[eo]);
    float c[3];
    for (int l = 0; l < 3; ++l) c[l] = rbf16(cosf(rbf16(bf2f(freq[l]) * a)));
    float s[4] = {0, 0, 0, 0};
    for (int r = 0; r < 8; ++r) {
        float df = rbf16(d - bf2f(centers[r]));
        float rb = rbf16(expf(rbf16(-10.f * rbf16(df * df))));
        for (int l = 0; l < 3; ++l) {
            float sb = rbf16(rb * c[l]);
            for (int k = 0; k < 4; ++k) s[k] += sb * bf2f(w_sbf[(r * 3 + l) * 4 + k]);
        }
    }
    float mm[4];
    for (int k = 0; k < 4; ++k) mm[k] = bf2f(m[ei * 4 + k]);
    for (int k = 0; k < 4; ++k) {
        float md = 0;
        for (int j = 0; j < 4; ++j) md += mm[j] * bf2f(w_down[j * 4 + k]);
        float x = rbf16(rbf16(md) * rbf16(s[k]));
        atomicAdd(&acc[eo * 4 + k], x);
    }
}

__global__ void k_fin(const u16* m, const u16* dist, const u16* centers, const u16* w_rbf,
                      const u16* w_up, const float* acc, __hip_bfloat16* out, int E) {
    int e = blockIdx.x * blockDim.x + threadIdx.x;
    if (e >= E) return;
    float d = bf2f(dist[e]);
    float g[4] = {0, 0, 0, 0};
    for (int r = 0; r < 8; ++r) {
        float df = rbf16(d - bf2f(centers[r]));
        float rb = rbf16(expf(rbf16(-10.f * rbf16(df * df))));
        for (int k = 0; k < 4; ++k) g[k] += rb * bf2f(w_rbf[r * 4 + k]);
    }
    float ag[4];
    for (int k = 0; k < 4; ++k) ag[k] = rbf16(acc[e * 4 + k]);
    for (int k = 0; k < 4; ++k) {
        float u = 0;
        for (int j = 0; j < 4; ++j) u += ag[j] * bf2f(w_up[j * 4 + k]);
        float y = rbf16(rbf16(u) * rbf16(g[k]));
        out[e * 4 + k] = __float2bfloat16(bf2f(m[e * 4 + k]) + y);
    }
}

}

extern "C" void kernel_launch(void* const* d_in, const int* in_sizes, int n_in,
                              void* d_out, int out_size, void* d_ws, size_t ws_size,
                              hipStream_t stream) {
    const u16* m = (const u16*)d_in[0];
    const u32* idx_in = (const u32*)d_in[1];
    const u32* idx_out = (const u32*)d_in[2];
    const u16* dist = (const u16*)d_in[3];
    const u16* angle = (const u16*)d_in[4];
    const u16* centers = (const u16*)d_in[5];
    const u16* freq = (const u16*)d_in[6];
    const u16* w_rbf = (const u16*)d_in[7];
    const u16* w_sbf = (const u16*)d_in[8];
    const u16* w_down = (const u16*)d_in[9];
    const u16* w_up = (const u16*)d_in[10];
    const int T = in_sizes[1], E = in_sizes[3];
    if (T != fast::kT || E != fast::kE || ws_size < fast::WS_NEED) {
        float* acc = (float*)d_ws;
        hipMemsetAsync(acc, 0, (size_t)E * 4 * sizeof(float), stream);
        generic::k_trip<<<(T + 255) / 256, 256, 0, stream>>>(m, (const int*)idx_in, (const int*)idx_out, dist, angle, centers, freq, w_sbf, w_down, acc, T);
        generic::k_fin<<<(E + 255) / 256, 256, 0, stream>>>(m, dist, centers, w_rbf, w_up, acc, (__hip_bfloat16*)d_out, E);
        return;
    }
    fast::k_bin<<<fast::NC, 1024, 0, stream>>>(idx_in, idx_out, angle, m, w_down, (char*)d_ws);
    fast::k_acc<<<fast::NB, 1024, 0, stream>>>(m, dist, centers, freq, w_rbf, w_sbf, w_up, (const char*)d_ws,
                                               (u16*)d_out);
}
```

```cpp
#include <hip/hip_runtime.h>
#include <hip/hip_bf16.h>
#include <stdint.h>

#define FUSED 1
#define IN_AUX 2
#define GDEPTH 3

typedef unsigned int u32;
typedef unsigned short u16;
typedef unsigned long long u64;
typedef u32 u32x2 __attribute__((ext_vector_type(2)));
typedef u32 u32x4 __attribute__((ext_vector_type(4)));
typedef float f32x4 __attribute__((ext_vector_type(4)));
typedef float f32x2 __attribute__((ext_vector_type(2)));
typedef const __attribute__((address_space(4))) u32* cptr;
typedef __amdgpu_buffer_rsrc_t rsrc_t;

static __device__ __forceinline__ float bf2f(u16 v) { return __uint_as_float(((u32)v) << 16); }
static __device__ __forceinline__ float lo16f(u32 v) { return __uint_as_float(v << 16); }
static __device__ __forceinline__ float hi16f(u32 v) { return __uint_as_float(v & 0xFFFF0000u); }
static __device__ __forceinline__ u32 pk_bf16(float a, float b) {
    typedef __bf16 bf16x2 __attribute__((ext_vector_type(2)));
    bf16x2 r = __builtin_convertvector((f32x2){a, b}, bf16x2);
    return __builtin_bit_cast(u32, r);
}
static __device__ __forceinline__ float rbf16(float x) { return lo16f(pk_bf16(x, x)); }
static __device__ __forceinline__ float cw(cptr p, int i) { u32 v = p[i >> 1]; return (i & 1) ? hi16f(v) : lo16f(v); }

namespace fast {
constexpr int kE = 160000, kT = 2560000;
constexpr int NWG = 256, EB = 625, NC = 250, S = 10240;
constexpr u32 MAGIC = 6871948u;
constexpr int NBIN = 160, BSTR = 520, SPILL = 1536;
constexpr u32 KMAGIC = 536870u;
constexpr int CHUNK_BYTES = S * 6;
constexpr size_t OFF_FLG = 4096;
constexpr size_t OFF_REC = OFF_FLG + (size_t)NWG * NWG * 16;
constexpr size_t OFF_DIAG = OFF_REC + (size_t)NWG * CHUNK_BYTES;
constexpr size_t WS_NEED = OFF_DIAG + 256 * 16 * 64;
constexpr int SC1 = FUSED ? 16 : 0;
constexpr int ST_AUX = 16;

constexpr int L_A = 0, A_STR = 640 * 16;
constexpr int L_ACC = 3 * A_STR, ACC_STR = 640 * 8;
constexpr int L_CNT = L_ACC + 2 * ACC_STR;
constexpr int L_BASE = L_CNT + 1024;
constexpr int L_BC = L_BASE + 1024;
constexpr int L_MISC = L_BC + 640;
constexpr int L_BIN = L_BC + 1024;
constexpr int L_SPILL = L_BIN + NBIN * BSTR;
constexpr int L_END = L_SPILL + SPILL * 8;
static_assert(L_END <= 163840 && CHUNK_BYTES <= NBIN * BSTR, "lds");

#define STAMP(i)
#define SB() __builtin_amdgcn_sched_barrier(0)

static __device__ __forceinline__ u32 lds_add(char* p, u32 v) {
    return __hip_atomic_fetch_add((u32*)p, v, __ATOMIC_RELAXED, __HIP_MEMORY_SCOPE_WORKGROUP);
}
static __device__ __forceinline__ u32 wave_scan_incl(u32 v) {
    v += __builtin_amdgcn_update_dpp(0u, v, 0x111, 0xf, 0xf, false);
    v += __builtin_amdgcn_update_dpp(0u, v, 0x112, 0xf, 0xf, false);
    v += __builtin_amdgcn_update_dpp(0u, v, 0x114, 0xf, 0xf, false);
    v += __builtin_amdgcn_update_dpp(0u, v, 0x118, 0xf, 0xf, false);
    v += __builtin_amdgcn_update_dpp(0u, v, 0x142, 0xa, 0xf, false);
    v += __builtin_amdgcn_update_dpp(0u, v, 0x143, 0xc, 0xf, false);
    return v;
}
static __device__ __forceinline__ float cos_rev(float x) {
    return __builtin_amdgcn_cosf(__builtin_amdgcn_fractf(x * 0.15915494309189535f));
}

struct EdgeC { f32x4 A0, A1, A2, g; };
static __device__ __forceinline__ EdgeC edge_coeffs(float d, cptr centers, cptr w_rbf, cptr w_sbf) {
    EdgeC e;
    e.A0 = e.A1 = e.A2 = e.g = (f32x4){0, 0, 0, 0};
#pragma unroll
    for (int q = 0; q < 8; ++q) {
        float df = rbf16(d - cw(centers, q));
        float rb = rbf16(__expf(rbf16(-10.f * rbf16(df * df))));
#pragma unroll
        for (int k = 0; k < 4; ++k) {
            e.A0[k] += rb * cw(w_sbf, (q * 3 + 0) * 4 + k);
            e.A1[k] += rb * cw(w_sbf, (q * 3 + 1) * 4 + k);
            e.A2[k] += rb * cw(w_sbf, (q * 3 + 2) * 4 + k);
            e.g[k] += rb * cw(w_rbf, q * 4 + k);
        }
    }
    return e;
}

struct Wd { f32x4 r0, r1, r2, r3; float f0, f1, f2; };

static __device__ __forceinline__ void msg_gate(u32 w0, u32 angw, u32x2 mr, const Wd& W, const char* smem, f32x4& md, f32x4& s) {
    float a = __uint_as_float(angw);
    u32 p01 = pk_bf16(W.f0 * a, W.f1 * a);
    u32 p2 = pk_bf16(W.f2 * a, W.f2 * a);
    float c0 = cos_rev(lo16f(p01)), c1 = cos_rev(hi16f(p01)), c2 = cos_rev(hi16f(p2));
    u32 l16 = (w0 >> 17) & 0x3FF0u;
    f32x4 A0 = *(const f32x4*)(smem + L_A + l16);
    f32x4 A1 = *(const f32x4*)(smem + L_A + A_STR + l16);
    f32x4 A2 = *(const f32x4*)(smem + L_A + 2 * A_STR + l16);
    s = A0 * c0 + A1 * c1 + A2 * c2;
    f32x4 t = W.r0 * lo16f(mr.x) + W.r1 * hi16f(mr.x) + W.r2 * lo16f(mr.y) + W.r3 * hi16f(mr.y);
    u32 q01 = pk_bf16(t.x, t.y), q23 = pk_bf16(t.z, t.w);
    md = (f32x4){lo16f(q01), hi16f(q01), lo16f(q23), hi16f(q23)};
}

static __device__ __forceinline__ void accum_fix(u32 w0, u32 angw, u32x2 mr, const Wd& W, char* smem) {
    f32x4 md, s;
    msg_gate(w0, angw, mr, W, smem, md, s);
    const float MG = 12582912.0f;
    f32x4 r = __builtin_elementwise_fma(md, s, (f32x4){MG, MG, MG, MG});
    const u32 C = 0x4B400000u;
    u32 q0 = __float_as_uint(r.x) - C, q2 = __float_as_uint(r.z) - C;
    u32 h1 = __float_as_uint(r.y) - C + (u32)((int)q0 >> 31);
    u32 h3 = __float_as_uint(r.w) - C + (u32)((int)q2 >> 31);
    u32 l8 = (w0 >> 18) & 0x1FF8u;
    __hip_atomic_fetch_add((u64*)(smem + L_ACC + l8), ((u64)h1 << 32) | q0, __ATOMIC_RELAXED, __HIP_MEMORY_SCOPE_WORKGROUP);
    __hip_atomic_fetch_add((u64*)(smem + L_ACC + ACC_STR + l8), ((u64)h3 << 32) | q2, __ATOMIC_RELAXED, __HIP_MEMORY_SCOPE_WORKGROUP);
}

static __device__ __forceinline__ void accum_slow(u32 w0, u32 angw, u32x2 mr, const Wd& W, char* smem) {
    f32x4 md, s;
    msg_gate(w0, angw, mr, W, smem, md, s);
    f32x4 x = md * s;
    double* acc = (double*)(smem + L_BIN + ((w0 >> 18) & 0x1FF8u));
    __hip_atomic_fetch_add(acc, (double)x.x, __ATOMIC_RELAXED, __HIP_MEMORY_SCOPE_WORKGROUP);
    __hip_atomic_fetch_add(acc + 640, (double)x.y, __ATOMIC_RELAXED, __HIP_MEMORY_SCOPE_WORKGROUP);
    __hip_atomic_fetch_add(acc + 1280, (double)x.z, __ATOMIC_RELAXED, __HIP_MEMORY_SCOPE_WORKGROUP);
    __hip_atomic_fetch_add(acc + 1920, (double)x.w, __ATOMIC_RELAXED, __HIP_MEMORY_SCOPE_WORKGROUP);
}

static __device__ __forceinline__ void sort_quad(u32x4 q, u32x2 an, u32 d, u32 len, char* smem) {
    u32 w0[4] = {q.x, q.y, q.z, q.w};
    u32 aw[4] = {an.x << 16, an.x & 0xFFFF0000u, an.y << 16, an.y & 0xFFFF0000u};
    u32 key[4], rk[4];
    bool v[4];
#pragma unroll
    for (int k = 0; k < 4; ++k) {
        v[k] = d + k < len;
        key[k] = __umulhi(w0[k] & 0x1FFFFFu, KMAGIC);
        rk[k] = 0;
        if (v[k]) rk[k] = lds_add(smem + L_BC + key[k] * 4, 8u);
    }
    bool ov = false;
#pragma unroll
    for (int k = 0; k < 4; ++k) {
        if (v[k]) *(u32x2*)(smem + L_BIN + key[k] * BSTR + min(rk[k], 512u)) = (u32x2){w0[k], aw[k]};
        ov |= rk[k] >= 512u;
    }
    if (ov) {
#pragma unroll
        for (int k = 0; k < 4; ++k)
            if (rk[k] >= 512u) {
                u32 p = lds_add(smem + L_MISC, 8u);
                if (p < SPILL * 8) *(u32x2*)(smem + L_SPILL + p) = (u32x2){w0[k], aw[k]};
            }
    }
}

template <int PH>
__global__ void __launch_bounds__(1024)
k_main(const u16* __restrict__ m, const u32* __restrict__ idx_in, const u32* __restrict__ idx_out,
       const u16* __restrict__ dist, const u16* __restrict__ angle, const u16* __restrict__ centers_,
       const u16* __restrict__ freq, const u16* __restrict__ w_rbf_, const u16* __restrict__ w_sbf_,
       const u16* __restrict__ w_down_, const u16* __restrict__ w_up_, u16* __restrict__ out, char* ws) {
    __shared__ __attribute__((aligned(16))) char smem[L_END];
    const int t = threadIdx.x, lane = t & 63;
    const int w = __builtin_amdgcn_readfirstlane(t >> 6);
    const int g = blockIdx.x;
    const int b = (g & 7) * 32 + (g >> 3);
    const bool prod = g < NC;
    cptr centers = (cptr)centers_, w_rbf = (cptr)w_rbf_, w_sbf = (cptr)w_sbf_, w_down = (cptr)w_down_, w_up = (cptr)w_up_;
    STAMP(PH == 2 ? 8 : 0);

    rsrc_t rs_ms = __builtin_amdgcn_make_buffer_rsrc((void*)(m + (size_t)b * EB * 4), 0, EB * 8, 0x00020000);
    rsrc_t rs_ds = __builtin_amdgcn_make_buffer_rsrc((void*)(dist + (size_t)b * EB), 0, EB * 2, 0x00020000);
    rsrc_t rs_fq = __builtin_amdgcn_make_buffer_rsrc((void*)freq, 0, 6, 0x00020000);
    rsrc_t rs_fl = __builtin_amdgcn_make_buffer_rsrc((void*)(ws + OFF_FLG), 0, NWG * NWG * 16, 0x00020000);
    rsrc_t rs_rc = __builtin_amdgcn_make_buffer_rsrc((void*)(ws + OFF_REC), 0, NWG * CHUNK_BYTES, 0x00020000);
    u32 tag = 0;
    if (PH == 0) tag = __builtin_amdgcn_raw_buffer_load_b32(__builtin_amdgcn_make_buffer_rsrc((void*)ws, 0, 16, 0x00020000), 0, 0, 16);
    u32x4 gr = {0, 0, 0, 0};
    const u32 fl_off = (u32)((w * 16 + (lane & 15)) * NWG + b) * 16;
    if (PH == 2) gr = __builtin_amdgcn_raw_buffer_load_b128(rs_fl, fl_off, 0, 0);
    u32 dv = 0, fq = 0;
    if (PH != 1) {
        dv = __builtin_amdgcn_raw_buffer_load_b16(rs_ds, t * 2, 0, 0);
        fq = __builtin_amdgcn_raw_buffer_load_b16(rs_fq, (lane & 3) * 2, 0, 0);
    }
    const u32x2 mrow = __builtin_amdgcn_raw_buffer_load_b64(rs_ms, t * 8, 0, 0);
    u32x4 o0, o1, i0, i1;
    u32x2 o2, i2, a0, a1;
    u32 a2;
    if (PH != 2) {
        const u32 nb = prod ? S * 4 : 0;
        const size_t cb = prod ? (size_t)g * S : 0;
        rsrc_t ro = __builtin_amdgcn_make_buffer_rsrc((void*)(idx_out + cb), 0, nb, 0x00020000);
        rsrc_t ri = __builtin_amdgcn_make_buffer_rsrc((void*)(idx_in + cb), 0, nb, 0x00020000);
        rsrc_t ra = __builtin_amdgcn_make_buffer_rsrc((void*)(angle + cb), 0, nb >> 1, 0x00020000);
        SB();
        o0 = __builtin_amdgcn_raw_buffer_load_b128(ro, t * 16, 0, IN_AUX);
        o1 = __builtin_amdgcn_raw_buffer_load_b128(ro, t * 16, 16384, IN_AUX);
        o2 = __builtin_amdgcn_raw_buffer_load_b64(ro, t * 8, 32768, IN_AUX);
        SB();
        i0 = __builtin_amdgcn_raw_buffer_load_b128(ri, t * 16, 0, IN_AUX);
        i1 = __builtin_amdgcn_raw_buffer_load_b128(ri, t * 16, 16384, IN_AUX);
        i2 = __builtin_amdgcn_raw_buffer_load_b64(ri, t * 8, 32768, IN_AUX);
        SB();
        a0 = __builtin_amdgcn_raw_buffer_load_b64(ra, t * 8, 0, IN_AUX);
        a1 = __builtin_amdgcn_raw_buffer_load_b64(ra, t * 8, 8192, IN_AUX);
        a2 = __builtin_amdgcn_raw_buffer_load_b32(ra, t * 4, 16384, IN_AUX);
        SB();
    }
    if (t < 192) *(u32x4*)(smem + L_CNT + t * 16) = (u32x4){0, 0, 0, 0};
    if (PH != 1 && t >= 256 && t < 896) *(u32x4*)(smem + L_ACC + (t - 256) * 16) = (u32x4){0, 0, 0, 0};
    __syncthreads();

    u32x4 rq[5];
    u32x2 ra_[5];
    u32 rd[5], rl[5];
    bool extra = false;
    auto issue_records = [&]() {
        const u32 seg = gr.y;
        const u32 need = (seg >> 16) + (seg & 3);
        extra = __builtin_amdgcn_ballot_w64(need > 64) != 0;
#pragma unroll
        for (int i = 0; i < 5; ++i) {
            const int sl = i < 4 ? i * 4 + (lane >> 4) : lane >> 2;
            const int j = i < 4 ? (lane & 15) : 16 + (lane & 3);
            u32 sg = __builtin_amdgcn_ds_bpermute(sl * 4, seg);
            rl[i] = sg >> 16;
            rd[i] = j * 4 - (sg & 3);
            u32 off = (u32)((w * 16 + sl) * CHUNK_BYTES + j * 24) + (sg & 0xFFF8u);
            off = (int)rd[i] < (int)rl[i] ? off : 0xFFFFFF00u;
            if (i < 4 || extra) {
                rq[i] = __builtin_amdgcn_raw_buffer_load_b128(rs_rc, off, 0, SC1);
                ra_[i] = __builtin_amdgcn_raw_buffer_load_b64(rs_rc, off + 16, 0, SC1);
            }
        }
        if (lane < 16) {
            lds_add(smem + L_MISC + 4, seg >> 16);
            __hip_atomic_fetch_max((u32*)(smem + L_MISC + 8), gr.w, __ATOMIC_RELAXED, __HIP_MEMORY_SCOPE_WORKGROUP);
            if (need > 80) *(u32*)(smem + L_MISC + 12) = 1;
        }
    };
    if (PH == 2) issue_records();

    f32x4 sa = {0, 0, 0, 0};
    u32x2 gp = {0, 0};
    if (w < 10) {
        if (PH != 1) {
            EdgeC e = edge_coeffs(lo16f(dv), centers, w_rbf, w_sbf);
            bool bad = false;
            f32x4 r;
#pragma unroll
            for (int k = 0; k < 4; ++k) {
                float s = fabsf(e.A0[k]) + fabsf(e.A1[k]) + fabsf(e.A2[k]);
                bad |= !(s < 0x1p120f);
                sa[k] = fmaxf(s, 0x1p-120f);
                r[k] = __builtin_amdgcn_rcpf(sa[k]);
            }
            *(f32x4*)(smem + L_A + t * 16) = e.A0 * r;
            *(f32x4*)(smem + L_A + A_STR + t * 16) = e.A1 * r;
            *(f32x4*)(smem + L_A + 2 * A_STR + t * 16) = e.A2 * r;
            gp = (u32x2){pk_bf16(e.g.x, e.g.y), pk_bf16(e.g.z, e.g.w)};
            if (bad && t < EB) *(u32*)(smem + L_MISC + 12) = 1;
        }
        if (PH != 2) {
            u32 mx = max(max(mrow.x & 0x7FFFu, (mrow.x >> 16) & 0x7FFFu), max(mrow.y & 0x7FFFu, (mrow.y >> 16) & 0x7FFFu));
#pragma unroll
            for (int o = 32; o > 0; o >>= 1) mx = max(mx, (u32)__shfl_xor((int)mx, o));
            if (lane == 0) __hip_atomic_fetch_max((u32*)(smem + L_MISC + 16), mx, __ATOMIC_RELAXED, __HIP_MEMORY_SCOPE_WORKGROUP);
        }
    }

    if (PH != 2) {
        u32 ov[10] = {o0.x, o0.y, o0.z, o0.w, o1.x, o1.y, o1.z, o1.w, o2.x, o2.y};
        u32 bk[10], ps[10];
        SB();
        if (prod) {
#pragma unroll
            for (int k = 0; k < 10; ++k) {
                u32 bb = __umulhi(ov[k] & 0xFFFFFFu, MAGIC);
                ov[k] = (u32)(__mul24((int)bb, -EB) + (int)ov[k]) << 21;
                bk[k] = bb * 4;
                ps[k] = lds_add(smem + L_CNT + bk[k], 2u);
            }
        }
        STAMP(1);
        __syncthreads();
        {
            u32x4 c = *(const u32x4*)(smem + L_CNT + lane * 16);
            u32 s1 = c.x, s2 = s1 + c.y, s3 = s2 + c.z, s4 = s3 + c.w;
            u32 ex = wave_scan_incl(s4) - s4;
            *(u32x4*)(smem + L_BASE + lane * 16) = (u32x4){ex, ex + s1, ex + s2, ex + s3};
        }
        __builtin_amdgcn_fence(__ATOMIC_RELEASE, "wavefront");
        __builtin_amdgcn_wave_barrier();
        __builtin_amdgcn_fence(__ATOMIC_ACQUIRE, "wavefront");
        if (prod) {
            u32 aa[10], ab[10];
#pragma unroll
            for (int k = 0; k < 10; ++k) {
                u32 p2 = *(const u32*)(smem + L_BASE + bk[k]) + ps[k];
                u32 r2 = p2 & 6u;
                aa[k] = p2 * 3 - r2;
                ab[k] = aa[k] - r2;
            }
            SB();
            u32 iv[10] = {i0.x, i0.y, i0.z, i0.w, i1.x, i1.y, i1.z, i1.w, i2.x, i2.y};
#pragma unroll
            for (int k = 0; k < 10; ++k) *(u32*)(smem + L_BIN + aa[k]) = (iv[k] << 3) | ov[k];
            SB();
            u32 av[5] = {a0.x, a0.y, a1.x, a1.y, a2};
#pragma unroll
            for (int k = 0; k < 10; ++k) *(u16*)(smem + L_BIN + 16 + ab[k]) = (u16)((k & 1) ? av[k >> 1] >> 16 : av[k >> 1]);
        }
        STAMP(2);
        __syncthreads();
        if (prod) {
            u32x4 v[4];
#pragma unroll
            for (int k = 0; k < 4; ++k)
                if (k < 3 || w < 12) v[k] = *(const u32x4*)(smem + L_BIN + k * 16384 + t * 16);
#pragma unroll
            for (int k = 0; k < 4; ++k)
                if (k < 3 || w < 12) __builtin_amdgcn_raw_buffer_store_b128(v[k], rs_rc, t * 16, g * CHUNK_BYTES + k * 16384, ST_AUX);
        }
        u32x4 fl;
        if (w < 4) {
            u32 st = *(const u32*)(smem + L_BASE + t * 4) >> 1, n = *(const u32*)(smem + L_CNT + t * 4) >> 1;
            fl = (u32x4){tag + 1, ((st >> 2) * 24) | (st & 3) | (n << 16), (tag + 1) ^ 0x9E3779B9u, *(const u32*)(smem + L_MISC + 16)};
        }
        STAMP(3);
        if (PH == 0) {
            asm volatile("s_waitcnt vmcnt(0)" ::: "memory");
            __syncthreads();
        }
        if (w < 4) __builtin_amdgcn_raw_buffer_store_b128(fl, rs_fl, t * 16, g * (NWG * 16), ST_AUX);
        STAMP(4);
    }
    if (PH == 1) return;

    if (PH == 0) {
#pragma unroll 1
        for (u32 spins = 0;; ++spins) {
            asm volatile("" ::: "memory");
            gr = __builtin_amdgcn_raw_buffer_load_b128(rs_fl, fl_off, 0, 16);
            bool ok = gr.x == tag + 1 && gr.z == ((tag + 1) ^ 0x9E3779B9u);
            if (__builtin_amdgcn_ballot_w64(!ok) == 0) break;
            if (spins > (1u << 16)) { gr = (u32x4){0, 0, 0, 0}; break; }
            __builtin_amdgcn_s_sleep(2);
        }
        __builtin_amdgcn_fence(__ATOMIC_ACQUIRE, "wavefront");
        STAMP(5);
        issue_records();
    }
#pragma unroll
    for (int i = 0; i < 4; ++i) sort_quad(rq[i], ra_[i], rd[i], rl[i], smem);
    if (extra) sort_quad(rq[4], ra_[4], rd[4], rl[4], smem);
    STAMP(6);
    __syncthreads();
    if (PH == 0 && g == 0 && t == 0)
        __builtin_amdgcn_raw_buffer_store_b32(tag + 1, __builtin_amdgcn_make_buffer_rsrc((void*)ws, 0, 16, 0x00020000), 0, 0, 16);

    const u32x4 misc = *(const u32x4*)(smem + L_MISC);
    const u32 nsp = __builtin_amdgcn_readfirstlane(misc.x) >> 3;
    bool slow = nsp > SPILL || __builtin_amdgcn_readfirstlane(misc.y) > 16383u || __builtin_amdgcn_readfirstlane(misc.w) != 0;
    Wd W;
    W.f0 = lo16f(__builtin_amdgcn_readlane(fq, 0));
    W.f1 = lo16f(__builtin_amdgcn_readlane(fq, 1));
    W.f2 = lo16f(__builtin_amdgcn_readlane(fq, 2));
    f32x4 inv;
    {
        const float mmax = lo16f(__builtin_amdgcn_readfirstlane(misc.z));
#pragma unroll
        for (int k = 0; k < 4; ++k) {
            float x0 = cw(w_down, k), x1 = cw(w_down, 4 + k), x2 = cw(w_down, 8 + k), x3 = cw(w_down, 12 + k);
            float bound = (fabsf(x0) + fabsf(x1) + fabsf(x2) + fabsf(x3)) * mmax * 1.02f;
            slow |= !(bound == 0.f || (bound > 0x1p-60f && bound < 0x1p60f && mmax > 0x1p-60f));
            int e = 17 - __builtin_amdgcn_frexp_expf(bound);
            e = slow ? 0 : e;
            W.r0[k] = ldexpf(x0, e); W.r1[k] = ldexpf(x1, e); W.r2[k] = ldexpf(x2, e); W.r3[k] = ldexpf(x3, e);
            inv[k] = ldexpf(1.f, -e);
        }
    }
    slow = __builtin_amdgcn_readfirstlane((int)slow);
    rsrc_t rs_m = __builtin_amdgcn_make_buffer_rsrc((void*)m, 0, kE * 8, 0x00020000);
    float ag[4];
    if (!slow) {
        constexpr int NS = 12;
        u32 cn = *(const u32*)(smem + L_BC + (w * 10 + min(lane, 9)) * 4) >> 3;
        u32x2 rec[NS], mg[NS];
        bool val[NS];
#pragma unroll
        for (int k = 0; k < NS; ++k) {
            if (k < 10) {
                rec[k] = *(const u32x2*)(smem + L_BIN + (w * 10 + k) * BSTR + lane * 8);
                val[k] = (u32)lane < (u32)__builtin_amdgcn_readlane(cn, k);
            } else {
                const u32 p = ((k - 10) * 16 + w) * 64 + lane;
                val[k] = p < nsp;
                rec[k] = *(const u32x2*)(smem + L_SPILL + min(p, (u32)SPILL - 1) * 8);
            }
        }
        auto live = [&](int k) { return k < 10 || (u32)(((k - 10) * 16 + w) * 64) < nsp; };
        auto gather = [&](int k) {
            if (live(k)) mg[k] = __builtin_amdgcn_raw_buffer_load_b64(rs_m, val[k] ? (rec[k].x & 0x1FFFFFu) : 0xFFFFFF00u, 0, 0);
        };
#pragma unroll
        for (int k = 0; k < GDEPTH; ++k) gather(k);
#pragma unroll
        for (int k = 0; k < NS; ++k) {
            SB();
            if (live(k) && val[k]) accum_fix(rec[k].x, rec[k].y, mg[k], W, smem);
            SB();
            if (k + GDEPTH < NS) gather(k + GDEPTH);
        }
        STAMP(7);
        __syncthreads();
        if (w < 10) {
            u64 s01 = *(const u64*)(smem + L_ACC + t * 8), s23 = *(const u64*)(smem + L_ACC + ACC_STR + t * 8);
            int q0 = (int)(u32)s01, q2 = (int)(u32)s23;
            int q1 = (int)(u32)(s01 >> 32) - (q0 >> 31), q3 = (int)(u32)(s23 >> 32) - (q2 >> 31);
            ag[0] = (float)q0 * inv[0] * sa[0]; ag[1] = (float)q1 * inv[1] * sa[1];
            ag[2] = (float)q2 * inv[2] * sa[2]; ag[3] = (float)q3 * inv[3] * sa[3];
        }
    } else {
#pragma unroll
        for (int k = 0; k < 4; ++k) { W.r0[k] = cw(w_down, k); W.r1[k] = cw(w_down, 4 + k); W.r2[k] = cw(w_down, 8 + k); W.r3[k] = cw(w_down, 12 + k); }
        if (w < 10) {
            EdgeC e = edge_coeffs(lo16f(dv), centers, w_rbf, w_sbf);
            *(f32x4*)(smem + L_A + t * 16) = e.A0;
            *(f32x4*)(smem + L_A + A_STR + t * 16) = e.A1;
            *(f32x4*)(smem + L_A + 2 * A_STR + t * 16) = e.A2;
            *(u32x4*)(smem + L_BIN + t * 32) = (u32x4){0, 0, 0, 0};
            *(u32x4*)(smem + L_BIN + t * 32 + 16) = (u32x4){0, 0, 0, 0};
        }
        __syncthreads();
        for (int s = 0; s < 16; ++s) {
            const u32 sg = __builtin_amdgcn_readlane(gr.y, s);
            const u32 len = sg >> 16, od = sg & 3;
            for (u32 q = 0; q * 4 < len + od; q += 64) {
                const u32 d = (q + lane) * 4 - od;
                u32 off = (u32)((w * 16 + s) * CHUNK_BYTES) + (sg & 0xFFF8u) + (q + lane) * 24;
                off = (int)d < (int)len ? off : 0xFFFFFF00u;
                u32x4 x = __builtin_amdgcn_raw_buffer_load_b128(rs_rc, off, 0, SC1);
                u32x2 y = __builtin_amdgcn_raw_buffer_load_b64(rs_rc, off + 16, 0, SC1);
                u32 w0[4] = {x.x, x.y, x.z, x.w};
                u32 aw[4] = {y.x << 16, y.x & 0xFFFF0000u, y.y << 16, y.y & 0xFFFF0000u};
#pragma unroll
                for (int k = 0; k < 4; ++k)
                    if (d + k < len) {
                        u32x2 mr = __builtin_amdgcn_raw_buffer_load_b64(rs_m, w0[k] & 0x1FFFFFu, 0, 0);
                        accum_slow(w0[k], aw[k], mr, W, smem);
                    }
            }
        }
        __syncthreads();
        if (w < 10) {
            const double* acc = (const double*)(smem + L_BIN) + t;
            ag[0] = (float)acc[0]; ag[1] = (float)acc[640]; ag[2] = (float)acc[1280]; ag[3] = (float)acc[1920];
        }
    }
    if (t < EB) {
        u32 p01 = pk_bf16(ag[0], ag[1]), p23 = pk_bf16(ag[2], ag[3]);
        float x[4] = {lo16f(p01), hi16f(p01), lo16f(p23), hi16f(p23)};
        float mm[4] = {lo16f(mrow.x), hi16f(mrow.x), lo16f(mrow.y), hi16f(mrow.y)};
        float gg[4] = {lo16f(gp.x), hi16f(gp.x), lo16f(gp.y), hi16f(gp.y)};
        float o[4];
#pragma unroll
        for (int k = 0; k < 4; ++k) {
            float u = 0.f;
#pragma unroll
            for (int q = 0; q < 4; ++q) u += x[q] * cw(w_up, q * 4 + k);
            o[k] = mm[k] + rbf16(rbf16(u) * gg[k]);
        }
        *(u32x2*)(out + ((size_t)b * EB + t) * 4) = (u32x2){pk_bf16(o[0], o[1]), pk_bf16(o[2], o[3])};
    }
    STAMP(9);
}
}

namespace generic {
__global__ void k_trip(const u16* m, const int* idx_in, const int* idx_out, const u16* dist,
                       const u16* angle, const u16* centers, const u16* freq,
                       const u16* w_sbf, const u16* w_down, float* acc, int T) {
    int t = blockIdx.x * blockDim.x + threadIdx.x;
    if (t >= T) return;
    int ei = idx_in[t], eo = idx_out[t];
    float a = bf2f(angle[t]);
    float d = bf2f(dist[eo]);
    float c[3];
    for (int l = 0; l < 3; ++l) c[l] = rbf16(cosf(rbf16(bf2f(freq[l]) * a)));
    float s[4] = {0, 0, 0, 0};
    for (int r = 0; r < 8; ++r) {
        float df = rbf16(d - bf2f(centers[r]));
        float rb = rbf16(expf(rbf16(-10.f * rbf16(df * df))));
        for (int l = 0; l < 3; ++l) {
            float sb = rbf16(rb * c[l]);
            for (int k = 0; k < 4; ++k) s[k] += sb * bf2f(w_sbf[(r * 3 + l) * 4 + k]);
        }
    }
    float mm[4];
    for (int k = 0; k < 4; ++k) mm[k] = bf2f(m[ei * 4 + k]);
    for (int k = 0; k < 4; ++k) {
        float md = 0;
        for (int j = 0; j < 4; ++j) md += mm[j] * bf2f(w_down[j * 4 + k]);
        float x = rbf16(rbf16(md) * rbf16(s[k]));
        atomicAdd(&acc[eo * 4 + k], x);
    }
}

__global__ void k_fin(const u16* m, const u16* dist, const u16* centers, const u16* w_rbf,
                      const u16* w_up, const float* acc, __hip_bfloat16* out, int E) {
    int e = blockIdx.x * blockDim.x + threadIdx.x;
    if (e >= E) return;
    float d = bf2f(dist[e]);
    float g[4] = {0, 0, 0, 0};
    for (int r = 0; r < 8; ++r) {
        float df = rbf16(d - bf2f(centers[r]));
        float rb = rbf16(expf(rbf16(-10.f * rbf16(df * df))));
        for (int k = 0; k < 4; ++k) g[k] += rb * bf2f(w_rbf[r * 4 + k]);
    }
    float ag[4];
    for (int k = 0; k < 4; ++k) ag[k] = rbf16(acc[e * 4 + k]);
    for (int k = 0; k < 4; ++k) {
        float u = 0;
        for (int j = 0; j < 4; ++j) u += ag[j] * bf2f(w_up[j * 4 + k]);
        float y = rbf16(rbf16(u) * rbf16(g[k]));
        out[e * 4 + k] = __float2bfloat16(bf2f(m[e * 4 + k]) + y);
    }
}

}

extern "C" void kernel_launch(void* const* d_in, const int* in_sizes, int n_in,
                              void* d_out, int out_size, void* d_ws, size_t ws_size,
                              hipStream_t stream) {
    const u16* m = (const u16*)d_in[0];
    const u32* idx_in = (const u32*)d_in[1];
    const u32* idx_out = (const u32*)d_in[2];
    const u16* dist = (const u16*)d_in[3];
    const u16* angle = (const u16*)d_in[4];
    const u16* centers = (const u16*)d_in[5];
    const u16* freq = (const u16*)d_in[6];
    const u16* w_rbf = (const u16*)d_in[7];
    const u16* w_sbf = (const u16*)d_in[8];
    const u16* w_down = (const u16*)d_in[9];
    const u16* w_up = (const u16*)d_in[10];
    const int T = in_sizes[1], E = in_sizes[3];
    if (T != fast::kT || E != fast::kE || ws_size < fast::WS_NEED) {
        float* acc = (float*)d_ws;
        hipMemsetAsync(acc, 0, (size_t)E * 4 * sizeof(float), stream);
        generic::k_trip<<<(T + 255) / 256, 256, 0, stream>>>(m, (const int*)idx_in, (const int*)idx_out, dist, angle, centers, freq, w_sbf, w_down, acc, T);
        generic::k_fin<<<(E + 255) / 256, 256, 0, stream>>>(m, dist, centers, w_rbf, w_up, acc, (__hip_bfloat16*)d_out, E);
        return;
    }
    fast::k_main<0><<<fast::NWG, 1024, 0, stream>>>(m, idx_in, idx_out, dist, angle, centers, freq, w_rbf, w_sbf, w_down, w_up, (u16*)d_out, (char*)d_ws);
}
```

```cpp
#include <hip/hip_runtime.h>
#include <hip/hip_bf16.h>
#include <stdint.h>

#define IN_AUX 2
#define GDEPTH 3
#define PF 0
#define WARM 1
#define CPF 1
#define EXP 0
#define PRIO 0

typedef unsigned int u32;
typedef unsigned short u16;
typedef unsigned long long u64;
typedef u32 u32x2 __attribute__((ext_vector_type(2)));
typedef u32 u32x4 __attribute__((ext_vector_type(4)));
typedef u32 u32x8 __attribute__((ext_vector_type(8)));
typedef u16 u16x2 __attribute__((ext_vector_type(2)));
typedef float f32x4 __attribute__((ext_vector_type(4)));
typedef float f32x2 __attribute__((ext_vector_type(2)));
typedef const __attribute__((address_space(4))) u32* cptr;
typedef __amdgpu_buffer_rsrc_t rsrc_t;

static __device__ __forceinline__ float bf2f(u16 v) { return __uint_as_float(((u32)v) << 16); }
static __device__ __forceinline__ float lo16f(u32 v) { return __uint_as_float(v << 16); }
static __device__ __forceinline__ float hi16f(u32 v) { return __uint_as_float(v & 0xFFFF0000u); }
static __device__ __forceinline__ u32 pk_bf16(float a, float b) {
    typedef __bf16 bf16x2 __attribute__((ext_vector_type(2)));
    bf16x2 r = __builtin_convertvector((f32x2){a, b}, bf16x2);
    return __builtin_bit_cast(u32, r);
}
static __device__ __forceinline__ float rbf16(float x) { return __uint_as_float(pk_bf16(0.f, x)); }
static __device__ __forceinline__ float cw(cptr p, int i) { u32 v = p[i >> 1]; return (i & 1) ? hi16f(v) : lo16f(v); }
static __device__ __forceinline__ u32 pkmax(u32 a, u32 b) {
    return __builtin_bit_cast(u32, __builtin_elementwise_max(__builtin_bit_cast(u16x2, a), __builtin_bit_cast(u16x2, b)));
}

namespace fast {
constexpr int kE = 160000, kT = 2560000;
constexpr int NWG = 256, EB = 625, NC = 256, SV = 10000, S = 10240;
constexpr u32 MAGIC = 6871948u;
constexpr int NBIN = 160, BSTR = 520, SPILL = 1536;
constexpr u32 KEY2 = 0x9E3779B9u;
constexpr size_t OFF_FLG = 4096;
constexpr size_t OFF_RW = OFF_FLG + (size_t)NWG * NWG * 16;
constexpr size_t OFF_RA = OFF_RW + (size_t)NC * S * 4;
constexpr size_t OFF_DIAG = OFF_RA + (size_t)NC * S * 2;
constexpr size_t WS_NEED = OFF_DIAG + 256 * 16 * 64;
constexpr int SC1 = 16;
constexpr u32 OOB = 0xFFFFFF00u;

constexpr int L_A = 0, A_STR = 640 * 16;
constexpr int L_ACC = 3 * A_STR, ACC_STR = 640 * 8;
constexpr int L_CNT = L_ACC + 2 * ACC_STR;
constexpr int L_BASE = L_CNT + 1024;
constexpr int L_BC = L_BASE + 1024;
constexpr int L_MISC = L_BC + 640;
constexpr int L_PAD = L_MISC + 32;
constexpr int L_BIN = L_BC + 1024;
constexpr int L_SPILL = L_BIN + NBIN * BSTR;
constexpr int L_END = L_SPILL + SPILL * 8;
constexpr int XB = L_BIN / 2;
static_assert(L_END <= 163840 && S * 6 <= NBIN * BSTR && L_BIN + S * 4 - XB < 65536, "lds");

#define STAMP(i)
#define SB() __builtin_amdgcn_sched_barrier(0)

static __device__ __forceinline__ u32 lds_add(char* p, u32 v) {
    return __hip_atomic_fetch_add((u32*)p, v, __ATOMIC_RELAXED, __HIP_MEMORY_SCOPE_WORKGROUP);
}
static __device__ __forceinline__ void stat_max(u32 st, u32 addr, bool on) {
    if (on) asm volatile("ds_max_u32 %0, %1\n\tds_max_u32 %0, %2 offset:4" :: "v"(addr), "v"(st & 0xFFFFu), "v"(st >> 16) : "memory");
}
static __device__ __forceinline__ u32 wave_scan_incl(u32 v) {
    v += __builtin_amdgcn_update_dpp(0u, v, 0x111, 0xf, 0xf, false);
    v += __builtin_amdgcn_update_dpp(0u, v, 0x112, 0xf, 0xf, false);
    v += __builtin_amdgcn_update_dpp(0u, v, 0x114, 0xf, 0xf, false);
    v += __builtin_amdgcn_update_dpp(0u, v, 0x118, 0xf, 0xf, false);
    v += __builtin_amdgcn_update_dpp(0u, v, 0x142, 0xa, 0xf, false);
    v += __builtin_amdgcn_update_dpp(0u, v, 0x143, 0xc, 0xf, false);
    return v;
}
static __device__ __forceinline__ u32 row_pkmax(u32 v) {
    v = pkmax(v, __builtin_amdgcn_update_dpp(0u, v, 0x111, 0xf, 0xf, false));
    v = pkmax(v, __builtin_amdgcn_update_dpp(0u, v, 0x112, 0xf, 0xf, false));
    v = pkmax(v, __builtin_amdgcn_update_dpp(0u, v, 0x114, 0xf, 0xf, false));
    v = pkmax(v, __builtin_amdgcn_update_dpp(0u, v, 0x118, 0xf, 0xf, false));
    return v;
}

struct EdgeC { f32x4 A0, A1, A2, g; };
static __device__ __forceinline__ EdgeC edge_coeffs(float d, cptr centers, cptr w_rbf, cptr w_sbf) {
    EdgeC e;
    e.A0 = e.A1 = e.A2 = e.g = (f32x4){0, 0, 0, 0};
#pragma unroll
    for (int q = 0; q < 8; ++q) {
        float df = rbf16(d - cw(centers, q));
        float rb = rbf16(__expf(rbf16(-10.f * rbf16(df * df))));
#pragma unroll
        for (int k = 0; k < 4; ++k) {
            e.A0[k] += rb * cw(w_sbf, (q * 3 + 0) * 4 + k);
            e.A1[k] += rb * cw(w_sbf, (q * 3 + 1) * 4 + k);
            e.A2[k] += rb * cw(w_sbf, (q * 3 + 2) * 4 + k);
            e.g[k] += rb * cw(w_rbf, q * 4 + k);
        }
    }
    return e;
}

constexpr float INV2PI = 0.15915494309189535f;

struct Fix { u32x8 wd; f32x2 f01; float f2; f32x2 mg01, mg23; u64 nb01, nb23; };

static __device__ __forceinline__ void accum_fix(u32 w0, u32 aw, u32x2 mr, const Fix& F, char* smem) {
    u32 l16, l8;
    asm("v_lshlrev_b16 %0, 4, %1" : "=v"(l16) : "v"(w0));
    asm("v_lshlrev_b16 %0, 3, %1" : "=v"(l8) : "v"(w0));
    const f32x4 A0 = *(const f32x4*)(smem + L_A + l16);
    const f32x4 A1 = *(const f32x4*)(smem + L_A + A_STR + l16);
    const f32x4 A2 = *(const f32x4*)(smem + L_A + 2 * A_STR + l16);
    const float a = __uint_as_float(aw);
    const f32x2 p01 = F.f01 * a;
    const f32x2 r01 = (f32x2){rbf16(p01.x), rbf16(p01.y)} * INV2PI;
    const float c0 = __builtin_amdgcn_cosf(r01.x), c1 = __builtin_amdgcn_cosf(r01.y);
    const float c2 = __builtin_amdgcn_cosf(rbf16(F.f2 * a) * INV2PI);
    float d0, d1, d2, d3;
    asm("v_dot2_f32_bf16 %0, %4, %6, 0\n\tv_dot2_f32_bf16 %1, %4, %7, 0\n\t"
        "v_dot2_f32_bf16 %2, %4, %8, 0\n\tv_dot2_f32_bf16 %3, %4, %9, 0\n\t"
        "v_dot2_f32_bf16 %0, %5, %10, %0\n\tv_dot2_f32_bf16 %1, %5, %11, %1\n\t"
        "v_dot2_f32_bf16 %2, %5, %12, %2\n\tv_dot2_f32_bf16 %3, %5, %13, %3\n\ts_nop 2"
        : "=&v"(d0), "=&v"(d1), "=&v"(d2), "=&v"(d3)
        : "v"(mr.x), "v"(mr.y), "s"(F.wd[0]), "s"(F.wd[1]), "s"(F.wd[2]), "s"(F.wd[3]),
          "s"(F.wd[4]), "s"(F.wd[5]), "s"(F.wd[6]), "s"(F.wd[7]));
    const f32x4 s = A0 * c0 + A1 * c1 + A2 * c2;
    const f32x2 q01 = __builtin_elementwise_fma((f32x2){d0, d1}, (f32x2){s.x, s.y}, F.mg01);
    const f32x2 q23 = __builtin_elementwise_fma((f32x2){d2, d3}, (f32x2){s.z, s.w}, F.mg23);
    __hip_atomic_fetch_add((u64*)(smem + L_ACC + l8), __builtin_bit_cast(u64, q01) + F.nb01, __ATOMIC_RELAXED, __HIP_MEMORY_SCOPE_WORKGROUP);
    __hip_atomic_fetch_add((u64*)(smem + L_ACC + ACC_STR + l8), __builtin_bit_cast(u64, q23) + F.nb23, __ATOMIC_RELAXED, __HIP_MEMORY_SCOPE_WORKGROUP);
}

static __device__ __forceinline__ float cos_any(float x) {
    return __builtin_amdgcn_cosf(__builtin_amdgcn_fractf(x * INV2PI));
}
static __device__ __forceinline__ void accum_slow(u32 w0, u32 aw, u32x2 mr, cptr w_down, float f0, float f1, float f2, char* smem) {
    const u32 l = w0 & 0x3FFu;
    const float a = __uint_as_float(aw);
    const float c0 = cos_any(rbf16(f0 * a)), c1 = cos_any(rbf16(f1 * a)), c2 = cos_any(rbf16(f2 * a));
    const f32x4 s = *(const f32x4*)(smem + L_A + l * 16) * c0 + *(const f32x4*)(smem + L_A + A_STR + l * 16) * c1 +
                    *(const f32x4*)(smem + L_A + 2 * A_STR + l * 16) * c2;
    double* acc = (double*)(smem + L_BIN + l * 8);
#pragma unroll
    for (int k = 0; k < 4; ++k) {
        float md = lo16f(mr.x) * cw(w_down, k) + hi16f(mr.x) * cw(w_down, 4 + k) + lo16f(mr.y) * cw(w_down, 8 + k) + hi16f(mr.y) * cw(w_down, 12 + k);
        __hip_atomic_fetch_add(acc + k * 640, (double)(rbf16(md) * s[k]), __ATOMIC_RELAXED, __HIP_MEMORY_SCOPE_WORKGROUP);
    }
}

static __device__ __forceinline__ void sort_quad(u32x4 q, u32x2 an, u32 d, u32 len, char* smem, int lane) {
    const u32 w0[4] = {q.x, q.y, q.z, q.w};
    u32 ka[4], rk[4];
    u64 mk[4];
#pragma unroll
    for (int k = 0; k < 4; ++k) {
        ka[k] = (w0[k] >> 24) << 2;
        mk[k] = __builtin_amdgcn_ballot_w64(d + k < len);
    }
    const u32 eight = 8;
    asm volatile("s_mov_b64 exec, %8\n\tds_add_rtn_u32 %0, %4, %12 offset:%13\n\t"
                 "s_mov_b64 exec, %9\n\tds_add_rtn_u32 %1, %5, %12 offset:%13\n\t"
                 "s_mov_b64 exec, %10\n\tds_add_rtn_u32 %2, %6, %12 offset:%13\n\t"
                 "s_mov_b64 exec, %11\n\tds_add_rtn_u32 %3, %7, %12 offset:%13\n\t"
                 "s_mov_b64 exec, -1"
                 : "=&v"(rk[0]), "=&v"(rk[1]), "=&v"(rk[2]), "=&v"(rk[3])
                 : "v"(ka[0]), "v"(ka[1]), "v"(ka[2]), "v"(ka[3]), "s"(mk[0]), "s"(mk[1]), "s"(mk[2]), "s"(mk[3]),
                   "v"(eight), "n"(L_BC)
                 : "memory");
    const u32x2 r[4] = {{w0[0], an.x << 16}, {w0[1], an.x & 0xFFFF0000u}, {w0[2], an.y << 16}, {w0[3], an.y & 0xFFFF0000u}};
    asm volatile("s_waitcnt lgkmcnt(0)" : "+v"(rk[0]), "+v"(rk[1]), "+v"(rk[2]), "+v"(rk[3]) :: "memory");
    u32 ad[4];
    u64 ov[4];
#pragma unroll
    for (int k = 0; k < 4; ++k) {
        ad[k] = __umul24(ka[k], BSTR / 4) + rk[k];
        ov[k] = __builtin_amdgcn_ballot_w64(rk[k] >= 512u) & mk[k];
        mk[k] &= ~ov[k];
    }
    asm volatile("s_mov_b64 exec, %8\n\tds_write_b64 %0, %4 offset:%12\n\t"
                 "s_mov_b64 exec, %9\n\tds_write_b64 %1, %5 offset:%12\n\t"
                 "s_mov_b64 exec, %10\n\tds_write_b64 %2, %6 offset:%12\n\t"
                 "s_mov_b64 exec, %11\n\tds_write_b64 %3, %7 offset:%12\n\t"
                 "s_mov_b64 exec, -1"
                 :: "v"(ad[0]), "v"(ad[1]), "v"(ad[2]), "v"(ad[3]), "v"(r[0]), "v"(r[1]), "v"(r[2]), "v"(r[3]),
                    "s"(mk[0]), "s"(mk[1]), "s"(mk[2]), "s"(mk[3]), "n"(L_BIN)
                 : "memory");
    if ((ov[0] | ov[1] | ov[2] | ov[3]) != 0) {
        const u32 n0 = __builtin_popcountll(ov[0]), n1 = n0 + __builtin_popcountll(ov[1]), n2 = n1 + __builtin_popcountll(ov[2]);
        const u32 n3 = n2 + __builtin_popcountll(ov[3]);
        u32 p = 0;
        if (lane == 0) p = lds_add(smem + L_MISC, n3);
        p = __builtin_amdgcn_readfirstlane(p);
        if (p + n3 <= SPILL) {
            const u32 pb[4] = {p, p + n0, p + n1, p + n2};
#pragma unroll
            for (int k = 0; k < 4; ++k)
                ad[k] = __builtin_amdgcn_mbcnt_hi((u32)(ov[k] >> 32), __builtin_amdgcn_mbcnt_lo((u32)ov[k], pb[k] + L_SPILL / 8)) * 8;
            asm volatile("s_mov_b64 exec, %8\n\tds_write_b64 %0, %4\n\t"
                         "s_mov_b64 exec, %9\n\tds_write_b64 %1, %5\n\t"
                         "s_mov_b64 exec, %10\n\tds_write_b64 %2, %6\n\t"
                         "s_mov_b64 exec, %11\n\tds_write_b64 %3, %7\n\t"
                         "s_mov_b64 exec, -1"
                         :: "v"(ad[0]), "v"(ad[1]), "v"(ad[2]), "v"(ad[3]), "v"(r[0]), "v"(r[1]), "v"(r[2]), "v"(r[3]),
                            "s"(ov[0]), "s"(ov[1]), "s"(ov[2]), "s"(ov[3])
                         : "memory");
        }
    }
}

__global__ void __launch_bounds__(1024)
k_main(const u16* p0, const u32* p1, const u32* p2, const u16* p3, const u16* p4, const u16* p5, const u16* p6,
       const u16* p7, const u16* p8, const u16* p9, const u16* p10, u16* p11, char* p12) {
    __shared__ __attribute__((aligned(16))) char smem[L_END];
    const __attribute__((address_space(4))) u64* kp = (const __attribute__((address_space(4))) u64*)__builtin_amdgcn_kernarg_segment_ptr();
    u64 P[13];
#pragma unroll
    for (int k = 0; k < 13; ++k) P[k] = kp[k];
    asm volatile("" : "+s"(P[0]), "+s"(P[1]), "+s"(P[2]), "+s"(P[3]), "+s"(P[4]), "+s"(P[5]), "+s"(P[6]), "+s"(P[7]), "+s"(P[8]),
                 "+s"(P[9]), "+s"(P[10]), "+s"(P[11]), "+s"(P[12]));
    const u16* m = (const u16*)P[0];
    const u32* idx_in = (const u32*)P[1];
    const u32* idx_out = (const u32*)P[2];
    const u16* dist = (const u16*)P[3];
    const u16* angle = (const u16*)P[4];
    const cptr centers = (cptr)P[5], freq = (cptr)P[6], w_rbf = (cptr)P[7], w_sbf = (cptr)P[8], w_down = (cptr)P[9], w_up = (cptr)P[10];
    u16* out = (u16*)P[11];
    char* ws = (char*)P[12];
    const int t = threadIdx.x, lane = t & 63;
    const int w = __builtin_amdgcn_readfirstlane(t >> 6);
    const int g = blockIdx.x;
    const int b = (g & 7) * 32 + (g >> 3);
    STAMP(0);

    const size_t cb = (size_t)g * SV;
    const rsrc_t ro = __builtin_amdgcn_make_buffer_rsrc((void*)(idx_out + cb), 0, SV * 4, 0x00020000);
    const u32x4 o0 = __builtin_amdgcn_raw_buffer_load_b128(ro, t * 16, 0, IN_AUX);
    const u32x4 o1 = __builtin_amdgcn_raw_buffer_load_b128(ro, t * 16, 16384, IN_AUX);
    const u32x2 o2 = __builtin_amdgcn_raw_buffer_load_b64(ro, t * 8, 32768, IN_AUX);
    SB();
    const rsrc_t rs_ms = __builtin_amdgcn_make_buffer_rsrc((void*)(m + (size_t)b * EB * 4), 0, EB * 8, 0x00020000);
    const rsrc_t rs_ds = __builtin_amdgcn_make_buffer_rsrc((void*)dist, 0, kE * 2, 0x00020000);
    const rsrc_t rs_ws = __builtin_amdgcn_make_buffer_rsrc((void*)ws, 0, (u32)OFF_RW, 0x00020000);
    const rsrc_t rs_rw = __builtin_amdgcn_make_buffer_rsrc((void*)(ws + OFF_RW), 0, NC * S * 4, 0x00020000);
    const rsrc_t rs_ra = __builtin_amdgcn_make_buffer_rsrc((void*)(ws + OFF_RA), 0, NC * S * 2, 0x00020000);
    const rsrc_t rs_out = __builtin_amdgcn_make_buffer_rsrc((void*)(out + (size_t)b * EB * 4), 0, EB * 8, 0x00020000);
    const rsrc_t rs_m = __builtin_amdgcn_make_buffer_rsrc((void*)m, 0, kE * 8, 0x00020000);
    const u32 tag_ = __builtin_amdgcn_raw_buffer_load_b32(rs_ws, 0, 0, SC1);
    const u32 dd_ = __builtin_amdgcn_raw_buffer_load_b32(rs_ds, ((b * EB + t) & ~1) * 2, 0, 0);
    const u32x2 mrow_ = __builtin_amdgcn_raw_buffer_load_b64(rs_ms, t * 8, 0, 0);
    u32 warm = 0;
    if (WARM && w == 15) {
        u64 p = (u64)w_sbf + (lane & 3) * 48;
        const u64 pp[6] = {(u64)centers, (u64)freq, (u64)w_rbf, (u64)w_down, (u64)w_up, (u64)(out + (size_t)b * EB * 4)};
#pragma unroll
        for (int k = 0; k < 6; ++k) p = lane == k + 4 ? pp[k] : p;
        if (CPF) p = lane >= 16 ? (u64)__builtin_amdgcn_s_getpc() + (((g >> 3) & 7) * 21 + lane - 16) * 128 : p;
        if (lane < 10 || (CPF && lane >= 16 && lane < 37)) warm = *(const __attribute__((address_space(1))) u32*)p;
    }
    SB();
    if (t < 192) *(u32x4*)(smem + L_CNT + t * 16) = (u32x4){t * 16 == L_PAD - L_CNT ? XB + 16384u : 0u, 0, 0, 0};
    if (t >= 256 && t < 896) *(u32x4*)(smem + L_ACC + (t - 256) * 16) = (u32x4){0, 0, 0, 0};
    __syncthreads();
    {
        const rsrc_t ri = __builtin_amdgcn_make_buffer_rsrc((void*)(idx_in + cb), 0, SV * 4, 0x00020000);
        const rsrc_t ra = __builtin_amdgcn_make_buffer_rsrc((void*)(angle + cb), 0, SV * 2, 0x00020000);
        const u32x4 i0 = __builtin_amdgcn_raw_buffer_load_b128(ri, t * 16, 0, IN_AUX);
        const u32x4 i1 = __builtin_amdgcn_raw_buffer_load_b128(ri, t * 16, 16384, IN_AUX);
        const u32x2 i2 = __builtin_amdgcn_raw_buffer_load_b64(ri, t * 8, 32768, IN_AUX);
        SB();
        const bool tail = t < (SV - 8192) / 2;
        u32 lo[10] = {o0.x, o0.y, o0.z, o0.w, o1.x, o1.y, o1.z, o1.w, o2.x, o2.y};
        u32 ps[10], bk[10];
        auto count = [&](int k) {
            u32 bb;
            asm("v_mul_hi_u32_u24 %0, %1, %2" : "=v"(bb) : "s"(MAGIC), "v"(lo[k]));
            bk[k] = bb * 4;
            ps[k] = lds_add(smem + L_CNT + bk[k], 2u);
            asm("v_mad_i32_i24 %0, %1, %2, %0" : "+v"(lo[k]) : "v"(bb), "s"(-EB));
        };
#pragma unroll
        for (int k = 0; k < 8; ++k) count(k);
        if (tail) { count(8); count(9); }
        SB();
        STAMP(1);
        const u32x2 a0 = __builtin_amdgcn_raw_buffer_load_b64(ra, t * 8, 0, IN_AUX);
        const u32x2 a1 = __builtin_amdgcn_raw_buffer_load_b64(ra, t * 8, 8192, IN_AUX);
        const u32 a2 = __builtin_amdgcn_raw_buffer_load_b32(ra, t * 4, 16384, IN_AUX);
        SB();
        __syncthreads();
        {
            const u32x4 c = *(const u32x4*)(smem + L_CNT + lane * 16);
            const u32 s1 = c.x, s2 = s1 + c.y, s3 = s2 + c.z, s4 = s3 + c.w;
            const u32 ex = wave_scan_incl(s4) - s4 + XB;
            *(u32x4*)(smem + L_BASE + lane * 16) = (u32x4){ex, ex + s1, ex + s2, ex + s3};
        }
        __builtin_amdgcn_fence(__ATOMIC_RELEASE, "wavefront");
        __builtin_amdgcn_wave_barrier();
        __builtin_amdgcn_fence(__ATOMIC_ACQUIRE, "wavefront");
        if (!tail) { bk[8] = bk[9] = L_PAD - L_BASE; ps[8] = t * 4; ps[9] = t * 4 + 2; }
#pragma unroll
        for (int k = 0; k < 10; ++k) ps[k] += *(const u32*)(smem + L_BASE + bk[k]);
        SB();
        const u32 iv[10] = {i0.x, i0.y, i0.z, i0.w, i1.x, i1.y, i1.z, i1.w, i2.x, i2.y};
#pragma unroll
        for (int k = 0; k < 10; ++k) *(u32*)(smem + ps[k] * 2) = (iv[k] << 14) | lo[k];
        STAMP(2);
        __syncthreads();
        {
            const rsrc_t rw = __builtin_amdgcn_make_buffer_rsrc((void*)(ws + OFF_RW + (size_t)g * (S * 4)), 0, SV * 4, 0x00020000);
            const u32x4 v0 = *(const u32x4*)(smem + L_BIN + t * 16), v1 = *(const u32x4*)(smem + L_BIN + 16384 + t * 16);
            const u32x2 v2 = *(const u32x2*)(smem + L_BIN + 32768 + t * 8);
            __builtin_amdgcn_raw_buffer_store_b128(v0, rw, t * 16, 0, SC1);
            __builtin_amdgcn_raw_buffer_store_b128(v1, rw, t * 16 + 16384, 0, SC1);
            __builtin_amdgcn_raw_buffer_store_b64(v2, rw, t * 8, 32768, SC1);
        }
        SB();
        const u32 av[5] = {a0.x, a0.y, a1.x, a1.y, a2};
#pragma unroll
        for (int k = 0; k < 10; ++k) *(u16*)(smem + ps[k] + (L_BIN + S * 4 - XB)) = (u16)((k & 1) ? av[k >> 1] >> 16 : av[k >> 1]);
        STAMP(8);
        __syncthreads();
        {
            const rsrc_t rn = __builtin_amdgcn_make_buffer_rsrc((void*)(ws + OFF_RA + (size_t)g * (S * 2)), 0, SV * 2, 0x00020000);
            const u32x4 v0 = *(const u32x4*)(smem + L_BIN + S * 4 + t * 16);
            const u32 v1 = *(const u32*)(smem + L_BIN + S * 4 + 16384 + t * 4);
            __builtin_amdgcn_raw_buffer_store_b128(v0, rn, t * 16, 0, SC1);
            __builtin_amdgcn_raw_buffer_store_b32(v1, rn, t * 4, 16384, SC1);
        }
        SB();
        u32 pf = 0;
        auto prefetch = [&]() {
            if (lane < 20) pf = __builtin_amdgcn_raw_buffer_load_b32(rs_m, (g >> 3) * 40000 + (w * 20 + lane) * 128, 0, 0);
        };
        if (PF == 1) prefetch();
        SB();
        STAMP(3);

        const float f0 = lo16f(freq[0]), f1 = hi16f(freq[0]), f2 = lo16f(freq[1]);
        const float dv = ((b + t) & 1) ? hi16f(dd_) : lo16f(dd_);
        {
            const u32 M = 0x7FFF7FFFu;
            const u32 amax = pkmax(pkmax(pkmax(a0.x & M, a0.y & M), pkmax(a1.x & M, a1.y & M)), a2 & M);
            const u32 am = max(amax & 0xFFFFu, amax >> 16);
            const bool far = !(lo16f(am) * (fabsf(f0) + fabsf(f1) + fabsf(f2)) < 1500.f);
            u32 rm = pkmax(mrow_.x & M, mrow_.y & M);
            rm = max(rm & 0xFFFFu, rm >> 16);
            stat_max(row_pkmax((far ? 0x8000u : 0u) | rm | (rm ? (0x7FFFu - rm) << 16 : 0u)), L_MISC + 8, (lane & 15) == 15);
        }
        f32x4 sa = {0, 0, 0, 0};
        u32x2 gp = {0, 0};
        if (w < 10) {
            const EdgeC e = edge_coeffs(dv, centers, w_rbf, w_sbf);
            bool bad = false;
            f32x4 r;
#pragma unroll
            for (int k = 0; k < 4; ++k) {
                const float s = fabsf(e.A0[k]) + fabsf(e.A1[k]) + fabsf(e.A2[k]);
                bad |= !(s < 0x1p120f);
                sa[k] = fmaxf(s, 0x1p-120f);
                r[k] = __builtin_amdgcn_rcpf(sa[k]);
            }
            *(f32x4*)(smem + L_A + t * 16) = e.A0 * r;
            *(f32x4*)(smem + L_A + A_STR + t * 16) = e.A1 * r;
            *(f32x4*)(smem + L_A + 2 * A_STR + t * 16) = e.A2 * r;
            gp = (u32x2){pk_bf16(e.g.x, e.g.y), pk_bf16(e.g.z, e.g.w)};
            if (bad && t < EB) *(u32*)(smem + L_MISC + 4) = 1;
        }
        Fix F;
        f32x4 cs;
        u32x8 wu;
        {
            const u32x8 d = *(const __attribute__((address_space(4))) u32x8*)w_down;
            const u32x8 u = *(const __attribute__((address_space(4))) u32x8*)w_up;
#pragma unroll
            for (int k = 0; k < 4; ++k) {
                const int h = k >> 1;
                const u32 x0 = d[h], x1 = d[2 + h], x2 = d[4 + h], x3 = d[6 + h];
                F.wd[k] = (k & 1) ? (x0 >> 16) | (x1 & 0xFFFF0000u) : (x0 & 0xFFFFu) | (x1 << 16);
                F.wd[4 + k] = (k & 1) ? (x2 >> 16) | (x3 & 0xFFFF0000u) : (x2 & 0xFFFFu) | (x3 << 16);
                cs[k] = (fabsf(lo16f(F.wd[k])) + fabsf(hi16f(F.wd[k])) + fabsf(lo16f(F.wd[4 + k])) + fabsf(hi16f(F.wd[4 + k]))) * 1.02f;
            }
            wu = u;
            asm volatile("" : "+s"(F.wd), "+s"(wu));
            F.f01 = (f32x2){f0, f1};
            F.f2 = f2;
        }
        SB();
        STAMP(11);
        asm volatile("s_waitcnt vmcnt(0) lgkmcnt(0)" :: "v"(warm) : "memory");
        STAMP(12);
        __syncthreads();
        const u32 tag = tag_ + 1;
        if (w < 4) {
            const u32 st = (*(const u32*)(smem + L_BASE + t * 4) - XB) >> 1, n = *(const u32*)(smem + L_CNT + t * 4) >> 1;
            const u32x2 own = *(const u32x2*)(smem + L_MISC + 8);
            __builtin_amdgcn_raw_buffer_store_b128((u32x4){tag, st | (n << 16), tag ^ KEY2, own.x | (own.y << 16)}, rs_ws,
                                                   (u32)OFF_FLG + g * (NWG * 16) + t * 16, 0, SC1);
        }
        if (PF == 2) prefetch();
        STAMP(4);

        u32x4 gr = {0, 0, 0, 0};
        u32 rdy = 0, done = 0, spins = 0;
        const u32 fl_off = (u32)OFF_FLG + (u32)((w * 16 + lane) * NWG + b) * 16;
        const u32 cj = w * 16 * S + (lane & 15) * 4;
        u32x4 rq[4];
        u32x2 rn[4];
        u32 rd[4], rl[4];
#pragma unroll
        for (int i = 0; i < 4; ++i) {
#pragma unroll 1
            while (__builtin_popcount(rdy & ~done) < 4) {
                asm volatile("" ::: "memory");
                if (rdy | spins) __builtin_amdgcn_s_sleep(1);
                if (lane < 16 && !((rdy >> lane) & 1)) gr = __builtin_amdgcn_raw_buffer_load_b128(rs_ws, fl_off, 0, SC1);
                rdy = (u32)__builtin_amdgcn_ballot_w64(gr.x == tag && gr.z == (tag ^ KEY2)) & 0xFFFFu;
                if (++spins > (1u << 16)) { gr.y = 0; rdy = 0xFFFFu; }
            }
            u32 av = rdy & ~done, pk = 0;
#pragma unroll
            for (int k = 0; k < 4; ++k) {
                pk |= (u32)__builtin_ctz(av) << (8 * k + 2);
                av &= av - 1;
            }
            done = rdy & ~av;
            const u32 sl4 = (pk >> ((lane >> 4) * 8)) & 0xFFu;
            const u32 sg = __builtin_amdgcn_ds_bpermute(sl4, gr.y);
            rl[i] = sg >> 16;
            rd[i] = (lane & 15) * 4 - (sg & 3);
            u32 off = (__umul24(sl4, S / 4) + cj + (sg & 0xFFFCu)) * 4;
            off = (int)rd[i] < (int)rl[i] ? off : OOB;
            rq[i] = __builtin_amdgcn_raw_buffer_load_b128(rs_rw, off, 0, SC1);
            rn[i] = __builtin_amdgcn_raw_buffer_load_b64(rs_ra, off >> 1, 0, SC1);
            if ((PF == 3 && i == 0) || (PF == 4 && i == 3) || (PF == 5 && i == 1)) prefetch();
            SB();
            if (i == 0) STAMP(5);
            if (i == 3) STAMP(10);
            if (i > 0) sort_quad(rq[i - 1], rn[i - 1], rd[i - 1], rl[i - 1], smem, lane);
            SB();
        }
        {
            const u32 need = (gr.y >> 16) + (gr.y & 3);
            stat_max(row_pkmax(gr.w), L_MISC + 16, lane == 15);
            if (need > 80) *(u32*)(smem + L_MISC + 4) = 1;
            sort_quad(rq[3], rn[3], rd[3], rl[3], smem, lane);
            if (__builtin_amdgcn_ballot_w64(need > 64) != 0) {
                const u32 sg = __builtin_amdgcn_ds_bpermute(lane & 60, gr.y);
                const u32 j4 = 64 + (lane & 3) * 4, d = j4 - (sg & 3), len = sg >> 16;
                u32 off = ((w * 16 + (lane >> 2)) * S + j4 + (sg & 0xFFFCu)) * 4;
                off = d < len ? off : OOB;
                const u32x4 q = __builtin_amdgcn_raw_buffer_load_b128(rs_rw, off, 0, SC1);
                const u32x2 an = __builtin_amdgcn_raw_buffer_load_b64(rs_ra, off >> 1, 0, SC1);
                sort_quad(q, an, d, len, smem, lane);
            }
        }
        STAMP(6);
        asm volatile("s_waitcnt lgkmcnt(0)" :: "v"(pf) : "memory");
        __syncthreads();
        STAMP(13);

        constexpr int NS = 12;
        u32x2 rec[NS], mg[NS];
        bool val[NS];
        const u32 cn = *(const u32*)(smem + L_BC + (w * 10 + min(lane, 9)) * 4);
        const u32x2 mi0 = *(const u32x2*)(smem + L_MISC), mi1 = *(const u32x2*)(smem + L_MISC + 16);
        const u32 l8 = lane * 8;
        auto fetch = [&](int k) {
            if (k < 10) rec[k] = *(const u32x2*)(smem + L_BIN + w * (10 * BSTR) + l8 + k * BSTR);
        };
#pragma unroll
        for (int k = 0; k < GDEPTH + 1; ++k) fetch(k);
        const u32 nsp = __builtin_amdgcn_readfirstlane(mi0.x);
        auto live = [&](int k) { return k < 10 || (u32)(((k - 10) * 16 + w) * 64) < nsp; };
        auto gather = [&](int k) {
            if (k < 10) {
                val[k] = l8 < (u32)__builtin_amdgcn_readlane(cn, k);
                mg[k] = __builtin_amdgcn_raw_buffer_load_b64(rs_m, val[k] ? (EXP == 1 ? l8 : rec[k].x >> 11) : OOB, 0, 0);
            } else if (live(k)) {
                const u32 p = ((k - 10) * 16 + w) * 64 + lane;
                val[k] = p < nsp;
                rec[k] = *(const u32x2*)(smem + L_SPILL + min(p, (u32)SPILL - 1) * 8);
                mg[k] = __builtin_amdgcn_raw_buffer_load_b64(rs_m, val[k] ? rec[k].x >> 11 : OOB, 0, 0);
            }
        };
#pragma unroll
        for (int k = 0; k < GDEPTH; ++k) gather(k);
        SB();
        if (g == 0 && t == 0) __builtin_amdgcn_raw_buffer_store_b32(tag, rs_ws, 0, 0, SC1);
        const u32 smax = __builtin_amdgcn_readfirstlane(mi1.x), smin = 0x7FFFu - __builtin_amdgcn_readfirstlane(mi1.y);
        bool slow = nsp > SPILL || __builtin_amdgcn_readfirstlane(mi0.y) != 0 || smax >= 0x7F80u || smin + (9u << 7) < smax;
        f32x4 inv;
        {
            const float mmax = lo16f(smax);
            float mgv[4];
#pragma unroll
            for (int k = 0; k < 4; ++k) {
                const float bound = cs[k] * mmax;
                slow |= !(bound == 0.f || (bound > 0x1p-60f && bound < 0x1p60f && mmax > 0x1p-60f));
                const int e = __builtin_amdgcn_frexp_expf(bound) - 17;
                mgv[k] = ldexpf(12582912.0f, e);
                inv[k] = ldexpf(1.f, e);
            }
            F.mg01 = (f32x2){mgv[0], mgv[1]};
            F.mg23 = (f32x2){mgv[2], mgv[3]};
            F.nb01 = 0 - __builtin_bit_cast(u64, F.mg01);
            F.nb23 = 0 - __builtin_bit_cast(u64, F.mg23);
        }
        slow = __builtin_amdgcn_readfirstlane((int)slow);
        float ag[4];
        u32 xacc = 0;
        if (!slow) {
#pragma unroll
            for (int k = 0; k < NS; ++k) {
                SB();
                if (PRIO && k == 0) __builtin_amdgcn_s_setprio(3);
                if (PRIO && k == 3) __builtin_amdgcn_s_setprio(2);
                if (PRIO && k == 6) __builtin_amdgcn_s_setprio(1);
                if (PRIO && k == 9) __builtin_amdgcn_s_setprio(0);
                if (k + GDEPTH + 1 < NS) fetch(k + GDEPTH + 1);
                if (EXP == 2) { if (live(k)) xacc ^= mg[k].x ^ mg[k].y; }
                else if (live(k) && val[k]) accum_fix(rec[k].x, rec[k].y, mg[k], F, smem);
                SB();
                if (k + GDEPTH < NS) gather(k + GDEPTH);
            }
            if (EXP == 2) asm volatile("" :: "v"(xacc));
            STAMP(7);
            __syncthreads();
            if (w < 10) {
                const u64 s01 = *(const u64*)(smem + L_ACC + t * 8), s23 = *(const u64*)(smem + L_ACC + ACC_STR + t * 8);
                const int q0 = (int)(u32)s01, q2 = (int)(u32)s23;
                const int q1 = (int)(u32)(s01 >> 32) - (q0 >> 31), q3 = (int)(u32)(s23 >> 32) - (q2 >> 31);
                ag[0] = (float)q0 * inv[0] * sa[0]; ag[1] = (float)q1 * inv[1] * sa[1];
                ag[2] = (float)q2 * inv[2] * sa[2]; ag[3] = (float)q3 * inv[3] * sa[3];
            }
        } else {
            __syncthreads();
            if (w < 10) {
                const EdgeC e = edge_coeffs(dv, centers, w_rbf, w_sbf);
                *(f32x4*)(smem + L_A + t * 16) = e.A0;
                *(f32x4*)(smem + L_A + A_STR + t * 16) = e.A1;
                *(f32x4*)(smem + L_A + 2 * A_STR + t * 16) = e.A2;
                *(u32x4*)(smem + L_BIN + t * 32) = (u32x4){0, 0, 0, 0};
                *(u32x4*)(smem + L_BIN + t * 32 + 16) = (u32x4){0, 0, 0, 0};
            }
            __syncthreads();
#pragma unroll 1
            for (int s = 0; s < 16; ++s) {
                const u32 sg = __builtin_amdgcn_readlane(gr.y, s);
                const u32 len = sg >> 16, p0 = (w * 16 + s) * S + (sg & 0xFFFFu);
#pragma unroll 1
                for (u32 q = 0; q < len; q += 64) {
                    if (q + lane < len) {
                        const u32 p = p0 + q + lane;
                        const u32 w0 = __builtin_amdgcn_raw_buffer_load_b32(rs_rw, p * 4, 0, SC1);
                        const u32 a2 = __builtin_amdgcn_raw_buffer_load_b32(rs_ra, (p >> 1) * 4, 0, SC1);
                        const u32x2 mr = __builtin_amdgcn_raw_buffer_load_b64(rs_m, w0 >> 11, 0, 0);
                        accum_slow(w0, (p & 1) ? a2 & 0xFFFF0000u : a2 << 16, mr, w_down, f0, f1, f2, smem);
                    }
                }
            }
            __syncthreads();
            if (w < 10) {
                const double* acc = (const double*)(smem + L_BIN) + t;
                ag[0] = (float)acc[0]; ag[1] = (float)acc[640]; ag[2] = (float)acc[1280]; ag[3] = (float)acc[1920];
            }
        }
        if (w < 10) {
            const u32 p01 = pk_bf16(ag[0], ag[1]), p23 = pk_bf16(ag[2], ag[3]);
            const float x[4] = {lo16f(p01), hi16f(p01), lo16f(p23), hi16f(p23)};
            const float mm[4] = {lo16f(mrow_.x), hi16f(mrow_.x), lo16f(mrow_.y), hi16f(mrow_.y)};
            const float gg[4] = {lo16f(gp.x), hi16f(gp.x), lo16f(gp.y), hi16f(gp.y)};
            float o[4];
#pragma unroll
            for (int k = 0; k < 4; ++k) {
                float u = 0.f;
#pragma unroll
                for (int q = 0; q < 4; ++q) u += x[q] * ((k & 1) ? hi16f(wu[q * 2 + (k >> 1)]) : lo16f(wu[q * 2 + (k >> 1)]));
                o[k] = mm[k] + rbf16(rbf16(u) * gg[k]);
            }
            __builtin_amdgcn_raw_buffer_store_b64((u32x2){pk_bf16(o[0], o[1]), pk_bf16(o[2], o[3])}, rs_out, t * 8, 0, SC1);
        }
    }
    STAMP(9);
}
}

namespace generic {
__global__ void k_trip(const u16* m, const int* idx_in, const int* idx_out, const u16* dist,
                       const u16* angle, const u16* centers, const u16* freq,
                       const u16* w_sbf, const u16* w_down, float* acc, int T) {
    int t = blockIdx.x * blockDim.x + threadIdx.x;
    if (t >= T) return;
    int ei = idx_in[t], eo = idx_out[t];
    float a = bf2f(angle[t]);
    float d = bf2f(dist[eo]);
    float c[3];
    for (int l = 0; l < 3; ++l) c[l] = rbf16(cosf(rbf16(bf2f(freq[l]) * a)));
    float s[4] = {0, 0, 0, 0};
    for (int r = 0; r < 8; ++r) {
        float df = rbf16(d - bf2f(centers[r]));
        float rb = rbf16(expf(rbf16(-10.f * rbf16(df * df))));
        for (int l = 0; l < 3; ++l) {
            float sb = rbf16(rb * c[l]);
            for (int k = 0; k < 4; ++k) s[k] += sb * bf2f(w_sbf[(r * 3 + l) * 4 + k]);
        }
    }
    float mm[4];
    for (int k = 0; k < 4; ++k) mm[k] = bf2f(m[ei * 4 + k]);
    for (int k = 0; k < 4; ++k) {
        float md = 0;
        for (int j = 0; j < 4; ++j) md += mm[j] * bf2f(w_down[j * 4 + k]);
        float x = rbf16(rbf16(md) * rbf16(s[k]));
        atomicAdd(&acc[eo * 4 + k], x);
    }
}

__global__ void k_fin(const u16* m, const u16* dist, const u16* centers, const u16* w_rbf,
                      const u16* w_up, const float* acc, __hip_bfloat16* out, int E) {
    int e = blockIdx.x * blockDim.x + threadIdx.x;
    if (e >= E) return;
    float d = bf2f(dist[e]);
    float g[4] = {0, 0, 0, 0};
    for (int r = 0; r < 8; ++r) {
        float df = rbf16(d - bf2f(centers[r]));
        float rb = rbf16(expf(rbf16(-10.f * rbf16(df * df))));
        for (int k = 0; k < 4; ++k) g[k] += rb * bf2f(w_rbf[r * 4 + k]);
    }
    float ag[4];
    for (int k = 0; k < 4; ++k) ag[k] = rbf16(acc[e * 4 + k]);
    for (int k = 0; k < 4; ++k) {
        float u = 0;
        for (int j = 0; j < 4; ++j) u += ag[j] * bf2f(w_up[j * 4 + k]);
        float y = rbf16(rbf16(u) * rbf16(g[k]));
        out[e * 4 + k] = __float2bfloat16(bf2f(m[e * 4 + k]) + y);
    }
}

}

extern "C" void kernel_launch(void* const* d_in, const int* in_sizes, int n_in,
                              void* d_out, int out_size, void* d_ws, size_t ws_size,
                              hipStream_t stream) {
    const u16* m = (const u16*)d_in[0];
    const u32* idx_in = (const u32*)d_in[1];
    const u32* idx_out = (const u32*)d_in[2];
    const u16* dist = (const u16*)d_in[3];
    const u16* angle = (const u16*)d_in[4];
    const u16* centers = (const u16*)d_in[5];
    const u16* freq = (const u16*)d_in[6];
    const u16* w_rbf = (const u16*)d_in[7];
    const u16* w_sbf = (const u16*)d_in[8];
    const u16* w_down = (const u16*)d_in[9];
    const u16* w_up = (const u16*)d_in[10];
    const int T = in_sizes[1], E = in_sizes[3];
    if (T != fast::kT || E != fast::kE || ws_size < fast::WS_NEED) {
        float* acc = (float*)d_ws;
        hipMemsetAsync(acc, 0, (size_t)E * 4 * sizeof(float), stream);
        generic::k_trip<<<(T + 255) / 256, 256, 0, stream>>>(m, (const int*)idx_in, (const int*)idx_out, dist, angle, centers, freq, w_sbf, w_down, acc, T);
        generic::k_fin<<<(E + 255) / 256, 256, 0, stream>>>(m, dist, centers, w_rbf, w_up, acc, (__hip_bfloat16*)d_out, E);
        return;
    }
    fast::k_main<<<fast::NWG, 1024, 0, stream>>>(m, idx_in, idx_out, dist, angle, centers, freq, w_rbf, w_sbf, w_down, w_up, (u16*)d_out, (char*)d_ws);
}
```

```cpp
#include <hip/hip_runtime.h>
#include <hip/hip_bf16.h>
#include <stdint.h>

#define IN_AUX 2
#define GDEPTH 3
#define NCOPY 32
#define CODE_LINES 16
#define PRIO 0
#define TOUCH 0
#define EXP 0

typedef unsigned int u32;
typedef unsigned short u16;
typedef unsigned long long u64;
typedef u32 u32x2 __attribute__((ext_vector_type(2)));
typedef u32 u32x4 __attribute__((ext_vector_type(4)));
typedef u32 u32x8 __attribute__((ext_vector_type(8)));
typedef u16 u16x2 __attribute__((ext_vector_type(2)));
typedef float f32x4 __attribute__((ext_vector_type(4)));
typedef float f32x2 __attribute__((ext_vector_type(2)));
typedef float f32x16 __attribute__((ext_vector_type(16)));
typedef short s16x4 __attribute__((ext_vector_type(4)));
typedef __bf16 bf16x8 __attribute__((ext_vector_type(8)));
typedef const __attribute__((address_space(4))) u32* cptr;
typedef __amdgpu_buffer_rsrc_t rsrc_t;

static __device__ __forceinline__ float bf2f(u16 v) { return __uint_as_float(((u32)v) << 16); }
static __device__ __forceinline__ float lo16f(u32 v) { return __uint_as_float(v << 16); }
static __device__ __forceinline__ float hi16f(u32 v) { return __uint_as_float(v & 0xFFFF0000u); }
static __device__ __forceinline__ u32 pk_bf16(float a, float b) {
    typedef __bf16 bf16x2 __attribute__((ext_vector_type(2)));
    bf16x2 r = __builtin_convertvector((f32x2){a, b}, bf16x2);
    return __builtin_bit_cast(u32, r);
}
static __device__ __forceinline__ float rbf16(float x) { return __uint_as_float(pk_bf16(0.f, x)); }
static __device__ __forceinline__ float cw(cptr p, int i) { u32 v = p[i >> 1]; return (i & 1) ? hi16f(v) : lo16f(v); }
static __device__ __forceinline__ u32 pkmax(u32 a, u32 b) {
    return __builtin_bit_cast(u32, __builtin_elementwise_max(__builtin_bit_cast(u16x2, a), __builtin_bit_cast(u16x2, b)));
}

namespace fast {
constexpr int kE = 160000, kT = 2560000;
constexpr int NWG = 256, EB = 625, NC = 256, SV = 10000, S = 10240;
constexpr u32 MAGIC = 6871948u;
constexpr int NBIN = 160, BSTR = 520, SPILL = 1536;
constexpr u32 KEY2 = 0x9E3779B9u;
constexpr size_t OFF_RDY = 4096;
constexpr size_t OFF_FLG = OFF_RDY + 64 * NWG * 8;
constexpr size_t OFF_RW = OFF_FLG + (size_t)NWG * NWG * 16;
constexpr size_t OFF_RA = OFF_RW + (size_t)NC * S * 4;
constexpr size_t OFF_DIAG = OFF_RA + (size_t)NC * S * 2;
constexpr size_t WS_NEED = OFF_DIAG + 256 * 16 * 64;
constexpr int SC1 = 16;
constexpr u32 OOB = 0xFFFFFF00u;

constexpr int L_A = 0, A_STR = 640 * 16;
constexpr int L_ACC = 3 * A_STR, ACC_STR = 640 * 8;
constexpr int L_CNT = L_ACC + 2 * ACC_STR;
constexpr int L_BASE = L_CNT + 1024;
constexpr int L_BC = L_BASE + 1024;
constexpr int L_MISC = L_BC + 640;
constexpr int L_PAD = L_MISC + 32;
constexpr int L_ZERO = L_PAD + 16;
constexpr int L_WD = L_BC + 704;
constexpr int L_WT = L_BC + 768;
constexpr int L_BIN = L_BC + 1024;
constexpr int L_SPILL = L_BIN + NBIN * BSTR;
constexpr int L_END = L_SPILL + SPILL * 8;
constexpr int XB = L_BIN / 2;
static_assert(L_END <= 163840 && S * 6 <= NBIN * BSTR && L_BIN + S * 4 - XB < 65536, "lds");

#define STAMP(i)
#define SB() __builtin_amdgcn_sched_barrier(0)

static __device__ __forceinline__ void setprio(int p) {
    if (p == 0) __builtin_amdgcn_s_setprio(0);
    if (p == 1) __builtin_amdgcn_s_setprio(1);
    if (p == 2) __builtin_amdgcn_s_setprio(2);
    if (p == 3) __builtin_amdgcn_s_setprio(3);
}
static __device__ __forceinline__ u32 lds_add(char* p, u32 v) {
    return __hip_atomic_fetch_add((u32*)p, v, __ATOMIC_RELAXED, __HIP_MEMORY_SCOPE_WORKGROUP);
}
static __device__ __forceinline__ void stat_max(u32 st, u32 addr, bool on) {
    if (on) asm volatile("ds_max_u32 %0, %1\n\tds_max_u32 %0, %2 offset:4" :: "v"(addr), "v"(st & 0xFFFFu), "v"(st >> 16) : "memory");
}
static __device__ __forceinline__ u32 wave_scan_incl(u32 v) {
    v += __builtin_amdgcn_update_dpp(0u, v, 0x111, 0xf, 0xf, false);
    v += __builtin_amdgcn_update_dpp(0u, v, 0x112, 0xf, 0xf, false);
    v += __builtin_amdgcn_update_dpp(0u, v, 0x114, 0xf, 0xf, false);
    v += __builtin_amdgcn_update_dpp(0u, v, 0x118, 0xf, 0xf, false);
    v += __builtin_amdgcn_update_dpp(0u, v, 0x142, 0xa, 0xf, false);
    v += __builtin_amdgcn_update_dpp(0u, v, 0x143, 0xc, 0xf, false);
    return v;
}
static __device__ __forceinline__ u32 row_pkmax(u32 v) {
    v = pkmax(v, __builtin_amdgcn_update_dpp(0u, v, 0x111, 0xf, 0xf, false));
    v = pkmax(v, __builtin_amdgcn_update_dpp(0u, v, 0x112, 0xf, 0xf, false));
    v = pkmax(v, __builtin_amdgcn_update_dpp(0u, v, 0x114, 0xf, 0xf, false));
    v = pkmax(v, __builtin_amdgcn_update_dpp(0u, v, 0x118, 0xf, 0xf, false));
    return v;
}

struct EdgeC { f32x4 A0, A1, A2, g; };
static __device__ __forceinline__ EdgeC edge_coeffs(float d, cptr centers, cptr w_rbf, cptr w_sbf) {
    EdgeC e;
    e.A0 = e.A1 = e.A2 = e.g = (f32x4){0, 0, 0, 0};
#pragma unroll
    for (int q = 0; q < 8; ++q) {
        float df = rbf16(d - cw(centers, q));
        float rb = rbf16(__expf(rbf16(-10.f * rbf16(df * df))));
#pragma unroll
        for (int k = 0; k < 4; ++k) {
            e.A0[k] += rb * cw(w_sbf, (q * 3 + 0) * 4 + k);
            e.A1[k] += rb * cw(w_sbf, (q * 3 + 1) * 4 + k);
            e.A2[k] += rb * cw(w_sbf, (q * 3 + 2) * 4 + k);
            e.g[k] += rb * cw(w_rbf, q * 4 + k);
        }
    }
    return e;
}

constexpr float INV2PI = 0.15915494309189535f;

struct Fix { f32x2 f01; float f2; f32x2 mg01, mg23; u64 nb01, nb23; };

static __device__ __forceinline__ f32x4 row_mat4(u32x2 col, u32x2 row) {
    return __builtin_amdgcn_mfma_f32_4x4x4bf16_1k(__builtin_bit_cast(s16x4, col), __builtin_bit_cast(s16x4, row), (f32x4){0, 0, 0, 0}, 0, 0, 0);
}
static __device__ __forceinline__ u64 add64(u64 a, u64 b) {
    u64 r;
    asm("v_lshl_add_u64 %0, %1, 0, %2" : "=v"(r) : "v"(a), "s"(b));
    return r;
}
static __device__ __forceinline__ void accum_fix(u32 w0, u32 aw, f32x4 d, const Fix& F, char* smem) {
    u32 l16, l8;
    asm("v_lshlrev_b16 %0, 4, %1" : "=v"(l16) : "v"(w0));
    asm("v_lshlrev_b16 %0, 3, %1" : "=v"(l8) : "v"(w0));
    const f32x4 A0 = *(const f32x4*)(smem + L_A + l16);
    const f32x4 A1 = *(const f32x4*)(smem + L_A + A_STR + l16);
    const f32x4 A2 = *(const f32x4*)(smem + L_A + 2 * A_STR + l16);
    const float a = __uint_as_float(aw);
    const f32x2 p01 = F.f01 * a;
    const f32x2 r01 = (f32x2){rbf16(p01.x), rbf16(p01.y)} * INV2PI;
    const float c0 = __builtin_amdgcn_cosf(r01.x), c1 = __builtin_amdgcn_cosf(r01.y);
    const float c2 = __builtin_amdgcn_cosf(rbf16(F.f2 * a) * INV2PI);
    const f32x4 s = A0 * c0 + A1 * c1 + A2 * c2;
    const f32x2 q01 = __builtin_elementwise_fma((f32x2){d.x, d.y}, (f32x2){s.x, s.y}, F.mg01);
    const f32x2 q23 = __builtin_elementwise_fma((f32x2){d.z, d.w}, (f32x2){s.z, s.w}, F.mg23);
    __hip_atomic_fetch_add((u64*)(smem + L_ACC + l8), add64(__builtin_bit_cast(u64, q01), F.nb01), __ATOMIC_RELAXED, __HIP_MEMORY_SCOPE_WORKGROUP);
    __hip_atomic_fetch_add((u64*)(smem + L_ACC + ACC_STR + l8), add64(__builtin_bit_cast(u64, q23), F.nb23), __ATOMIC_RELAXED, __HIP_MEMORY_SCOPE_WORKGROUP);
}

static __device__ __forceinline__ float cos_any(float x) {
    return __builtin_amdgcn_cosf(__builtin_amdgcn_fractf(x * INV2PI));
}
static __device__ __forceinline__ void accum_slow(u32 w0, u32 aw, u32x2 mr, cptr w_down, float f0, float f1, float f2, char* smem) {
    const u32 l = w0 & 0x3FFu;
    const float a = __uint_as_float(aw);
    const float c0 = cos_any(rbf16(f0 * a)), c1 = cos_any(rbf16(f1 * a)), c2 = cos_any(rbf16(f2 * a));
    const f32x4 s = *(const f32x4*)(smem + L_A + l * 16) * c0 + *(const f32x4*)(smem + L_A + A_STR + l * 16) * c1 +
                    *(const f32x4*)(smem + L_A + 2 * A_STR + l * 16) * c2;
    double* acc = (double*)(smem + L_BIN + l * 8);
#pragma unroll
    for (int k = 0; k < 4; ++k) {
        float md = lo16f(mr.x) * cw(w_down, k) + hi16f(mr.x) * cw(w_down, 4 + k) + lo16f(mr.y) * cw(w_down, 8 + k) + hi16f(mr.y) * cw(w_down, 12 + k);
        __hip_atomic_fetch_add(acc + k * 640, (double)(rbf16(md) * s[k]), __ATOMIC_RELAXED, __HIP_MEMORY_SCOPE_WORKGROUP);
    }
}

static __device__ __forceinline__ void sort_quad(u32x4 q, u32x2 an, u32 d, u32 len, char* smem, int lane) {
    const u32 w0[4] = {q.x, q.y, q.z, q.w};
    u32 ka[4], rk[4];
    u64 mk[4];
#pragma unroll
    for (int k = 0; k < 4; ++k) {
        ka[k] = (w0[k] >> 24) << 2;
        mk[k] = __builtin_amdgcn_ballot_w64(d + k < len);
    }
    const u32 eight = 8;
    asm volatile("s_mov_b64 exec, %8\n\tds_add_rtn_u32 %0, %4, %12 offset:%13\n\t"
                 "s_mov_b64 exec, %9\n\tds_add_rtn_u32 %1, %5, %12 offset:%13\n\t"
                 "s_mov_b64 exec, %10\n\tds_add_rtn_u32 %2, %6, %12 offset:%13\n\t"
                 "s_mov_b64 exec, %11\n\tds_add_rtn_u32 %3, %7, %12 offset:%13\n\t"
                 "s_mov_b64 exec, -1"
                 : "=&v"(rk[0]), "=&v"(rk[1]), "=&v"(rk[2]), "=&v"(rk[3])
                 : "v"(ka[0]), "v"(ka[1]), "v"(ka[2]), "v"(ka[3]), "s"(mk[0]), "s"(mk[1]), "s"(mk[2]), "s"(mk[3]),
                   "v"(eight), "n"(L_BC)
                 : "memory");
    const u32x2 r[4] = {{w0[0], an.x << 16}, {w0[1], an.x & 0xFFFF0000u}, {w0[2], an.y << 16}, {w0[3], an.y & 0xFFFF0000u}};
    asm volatile("s_waitcnt lgkmcnt(0)" : "+v"(rk[0]), "+v"(rk[1]), "+v"(rk[2]), "+v"(rk[3]) :: "memory");
    u32 ad[4];
    u64 ov[4];
#pragma unroll
    for (int k = 0; k < 4; ++k) {
        ad[k] = __umul24(ka[k], BSTR / 4) + rk[k];
        ov[k] = __builtin_amdgcn_ballot_w64(rk[k] >= 512u) & mk[k];
        mk[k] &= ~ov[k];
    }
    asm volatile("s_mov_b64 exec, %8\n\tds_write_b64 %0, %4 offset:%12\n\t"
                 "s_mov_b64 exec, %9\n\tds_write_b64 %1, %5 offset:%12\n\t"
                 "s_mov_b64 exec, %10\n\tds_write_b64 %2, %6 offset:%12\n\t"
                 "s_mov_b64 exec, %11\n\tds_write_b64 %3, %7 offset:%12\n\t"
                 "s_mov_b64 exec, -1"
                 :: "v"(ad[0]), "v"(ad[1]), "v"(ad[2]), "v"(ad[3]), "v"(r[0]), "v"(r[1]), "v"(r[2]), "v"(r[3]),
                    "s"(mk[0]), "s"(mk[1]), "s"(mk[2]), "s"(mk[3]), "n"(L_BIN)
                 : "memory");
    if ((ov[0] | ov[1] | ov[2] | ov[3]) != 0) {
        const u32 n0 = __builtin_popcountll(ov[0]), n1 = n0 + __builtin_popcountll(ov[1]), n2 = n1 + __builtin_popcountll(ov[2]);
        const u32 n3 = n2 + __builtin_popcountll(ov[3]);
        u32 p = 0;
        if (lane == 0) p = lds_add(smem + L_MISC, n3);
        p = __builtin_amdgcn_readfirstlane(p);
        if (p + n3 <= SPILL) {
            const u32 pb[4] = {p, p + n0, p + n1, p + n2};
#pragma unroll
            for (int k = 0; k < 4; ++k)
                ad[k] = __builtin_amdgcn_mbcnt_hi((u32)(ov[k] >> 32), __builtin_amdgcn_mbcnt_lo((u32)ov[k], pb[k] + L_SPILL / 8)) * 8;
            asm volatile("s_mov_b64 exec, %8\n\tds_write_b64 %0, %4\n\t"
                         "s_mov_b64 exec, %9\n\tds_write_b64 %1, %5\n\t"
                         "s_mov_b64 exec, %10\n\tds_write_b64 %2, %6\n\t"
                         "s_mov_b64 exec, %11\n\tds_write_b64 %3, %7\n\t"
                         "s_mov_b64 exec, -1"
                         :: "v"(ad[0]), "v"(ad[1]), "v"(ad[2]), "v"(ad[3]), "v"(r[0]), "v"(r[1]), "v"(r[2]), "v"(r[3]),
                            "s"(ov[0]), "s"(ov[1]), "s"(ov[2]), "s"(ov[3])
                         : "memory");
        }
    }
}

__global__ void __launch_bounds__(1024)
k_main(const u16* p0, const u32* p1, const u32* p2, const u16* p3, const u16* p4, const u16* p5, const u16* p6,
       const u16* p7, const u16* p8, const u16* p9, const u16* p10, u16* p11, char* p12) {
    __shared__ __attribute__((aligned(16))) char smem[L_END];
    const __attribute__((address_space(4))) u64* kp = (const __attribute__((address_space(4))) u64*)__builtin_amdgcn_kernarg_segment_ptr();
    u64 P[13];
#pragma unroll
    for (int k = 0; k < 13; ++k) P[k] = kp[k];
    asm volatile("" : "+s"(P[0]), "+s"(P[1]), "+s"(P[2]), "+s"(P[3]), "+s"(P[4]), "+s"(P[5]), "+s"(P[6]), "+s"(P[7]), "+s"(P[8]),
                 "+s"(P[9]), "+s"(P[10]), "+s"(P[11]), "+s"(P[12]));
    const u16* m = (const u16*)P[0];
    const u32* idx_in = (const u32*)P[1];
    const u32* idx_out = (const u32*)P[2];
    const u16* dist = (const u16*)P[3];
    const u16* angle = (const u16*)P[4];
    const cptr centers = (cptr)P[5], freq = (cptr)P[6], w_rbf = (cptr)P[7], w_sbf = (cptr)P[8], w_down = (cptr)P[9], w_up = (cptr)P[10];
    u16* out = (u16*)P[11];
    char* ws = (char*)P[12];
    const int t = threadIdx.x, lane = t & 63;
    const int w = __builtin_amdgcn_readfirstlane(t >> 6);
    const int g = blockIdx.x;
    const int b = (g & 7) * 32 + (g >> 3);
    STAMP(0);

    const size_t cb = (size_t)g * SV;
    const rsrc_t ro = __builtin_amdgcn_make_buffer_rsrc((void*)(idx_out + cb), 0, SV * 4, 0x00020000);
    const u32x4 o0 = __builtin_amdgcn_raw_buffer_load_b128(ro, t * 16, 0, IN_AUX);
    const u32x4 o1 = __builtin_amdgcn_raw_buffer_load_b128(ro, t * 16, 16384, IN_AUX);
    const u32x2 o2 = __builtin_amdgcn_raw_buffer_load_b64(ro, t * 8, 32768, IN_AUX);
    SB();
    const rsrc_t rs_ms = __builtin_amdgcn_make_buffer_rsrc((void*)(m + (size_t)b * EB * 4), 0, EB * 8, 0x00020000);
    const rsrc_t rs_ds = __builtin_amdgcn_make_buffer_rsrc((void*)dist, 0, kE * 2, 0x00020000);
    const rsrc_t rs_ws = __builtin_amdgcn_make_buffer_rsrc((void*)ws, 0, (u32)OFF_RW, 0x00020000);
    const rsrc_t rs_rw = __builtin_amdgcn_make_buffer_rsrc((void*)(ws + OFF_RW), 0, NC * S * 4, 0x00020000);
    const rsrc_t rs_ra = __builtin_amdgcn_make_buffer_rsrc((void*)(ws + OFF_RA), 0, NC * S * 2, 0x00020000);
    const rsrc_t rs_out = __builtin_amdgcn_make_buffer_rsrc((void*)(out + (size_t)b * EB * 4), 0, EB * 8, 0x00020000);
    const rsrc_t rs_m = __builtin_amdgcn_make_buffer_rsrc((void*)m, 0, kE * 8, 0x00020000);
    const u32 tag_ = __builtin_amdgcn_raw_buffer_load_b32(rs_ws, 0, 0, SC1);
    const u32 dd_ = __builtin_amdgcn_raw_buffer_load_b32(rs_ds, ((b * EB + t) & ~1) * 2, 0, 0);
    const u32x2 mrow_ = __builtin_amdgcn_raw_buffer_load_b64(rs_ms, t * 8, 0, 0);
    u32 small = 0;
    if (w >= 14) {
        u64 p;
        if (w == 15) {
            p = lane < 48 ? (u64)w_sbf + lane * 4 : (u64)w_rbf + (lane - 48) * 4;
        } else {
            p = lane < 8 ? (u64)w_down + lane * 4 : (u64)w_up + (lane & 7) * 4;
            p = lane == 16 ? (u64)centers : p;
            p = lane == 17 ? (u64)freq : p;
            p = lane == 18 ? (u64)(out + (size_t)b * EB * 4) : p;
            if (TOUCH & 1) {
                p = lane == 19 ? (u64)(ws + OFF_RW + (size_t)g * (S * 4)) : p;
                p = lane == 20 ? (u64)(ws + OFF_RW + (size_t)g * (S * 4) + (SV * 4 - 4)) : p;
                p = lane == 21 ? (u64)(ws + OFF_RA + (size_t)g * (S * 2)) : p;
                p = lane == 22 ? (u64)(ws + OFF_RA + (size_t)g * (S * 2) + (SV * 2 - 4)) : p;
                p = lane == 23 ? (u64)(ws + OFF_FLG + (size_t)g * (NWG * 16)) : p;
            }
            if (TOUCH & 2) p = lane >= 48 ? (u64)ws + (lane - 48) * (u32)(((OFF_DIAG - 4096) / 15) & ~127ull) + g * 128 % 4096 : p;
            if (TOUCH & 4) p = (lane == 24 || lane == 25) ? (u64)m + (lane - 24) * (kE * 8 - 4) : p;
            p = lane >= 32 && lane < 32 + CODE_LINES ? (u64)__builtin_amdgcn_s_getpc() + (((g >> 3) & 7) * CODE_LINES + lane - 32) * 128 : p;
        }
        small = *(const __attribute__((address_space(1))) u32*)p;
    }
    SB();
    if (t < 192) *(u32x4*)(smem + L_CNT + t * 16) = (u32x4){t * 16 == L_PAD - L_CNT ? XB + 16384u : 0u, 0, 0, 0};
    if (t >= 256 && t < 896) *(u32x4*)(smem + L_ACC + (t - 256) * 16) = (u32x4){0, 0, 0, 0};
    __syncthreads();
    {
        const rsrc_t ri = __builtin_amdgcn_make_buffer_rsrc((void*)(idx_in + cb), 0, SV * 4, 0x00020000);
        const rsrc_t ra = __builtin_amdgcn_make_buffer_rsrc((void*)(angle + cb), 0, SV * 2, 0x00020000);
        const u32x4 i0 = __builtin_amdgcn_raw_buffer_load_b128(ri, t * 16, 0, IN_AUX);
        const u32x4 i1 = __builtin_amdgcn_raw_buffer_load_b128(ri, t * 16, 16384, IN_AUX);
        const u32x2 i2 = __builtin_amdgcn_raw_buffer_load_b64(ri, t * 8, 32768, IN_AUX);
        SB();
        const bool tail = t < (SV - 8192) / 2;
        u32 lo[10] = {o0.x, o0.y, o0.z, o0.w, o1.x, o1.y, o1.z, o1.w, o2.x, o2.y};
        u32 ps[10], bk[10];
        auto count = [&](int k) {
            u32 bb;
            asm("v_mul_hi_u32_u24 %0, %1, %2" : "=v"(bb) : "s"(MAGIC), "v"(lo[k]));
            bk[k] = bb * 4;
            ps[k] = lds_add(smem + L_CNT + bk[k], 2u);
            asm("v_mad_i32_i24 %0, %1, %2, %0" : "+v"(lo[k]) : "v"(bb), "s"(-EB));
        };
#pragma unroll
        for (int k = 0; k < 8; ++k) count(k);
        if (tail) { count(8); count(9); }
        SB();
        STAMP(1);
        const u32x2 a0 = __builtin_amdgcn_raw_buffer_load_b64(ra, t * 8, 0, IN_AUX);
        const u32x2 a1 = __builtin_amdgcn_raw_buffer_load_b64(ra, t * 8, 8192, IN_AUX);
        const u32 a2 = __builtin_amdgcn_raw_buffer_load_b32(ra, t * 4, 16384, IN_AUX);
        SB();
        if (w >= 14) {
            u32 ad;
            if (w == 15) {
                const u32 row = lane >> 1, r = lane < 48 ? (row * 43) >> 7 : row - 24;
                ad = L_WT + (lane < 48 ? row - r * 3 : 3) * 64 + (lane & 1) * 32 + r * 2;
                *(u16*)(smem + ad) = (u16)small;
                *(u16*)(smem + ad + 16) = (u16)(small >> 16);
            } else if (lane < 16) {
                ad = L_WD + (lane >> 3) * 32 + (lane & 1) * 16 + ((lane >> 1) & 3) * 2;
                *(u16*)(smem + ad) = (u16)small;
                *(u16*)(smem + ad + 8) = (u16)(small >> 16);
            }
        }
        if (w < 10) {
            const u32 M = 0x7FFF7FFFu;
            u32 rm = pkmax(mrow_.x & M, mrow_.y & M);
            rm = max(rm & 0xFFFFu, rm >> 16);
            stat_max(row_pkmax(rm | (rm ? (0x7FFFu - rm) << 16 : 0u)), L_MISC + 8, (lane & 15) == 15);
        }
        asm volatile("s_waitcnt lgkmcnt(0)" ::: "memory");
        __syncthreads();
        {
            const u32x4 c = *(const u32x4*)(smem + L_CNT + lane * 16);
            const u32 s1 = c.x, s2 = s1 + c.y, s3 = s2 + c.z, s4 = s3 + c.w;
            const u32 ex = wave_scan_incl(s4) - s4 + XB;
            *(u32x4*)(smem + L_BASE + lane * 16) = (u32x4){ex, ex + s1, ex + s2, ex + s3};
        }
        __builtin_amdgcn_fence(__ATOMIC_RELEASE, "wavefront");
        __builtin_amdgcn_wave_barrier();
        __builtin_amdgcn_fence(__ATOMIC_ACQUIRE, "wavefront");
        if (!tail) { bk[8] = bk[9] = L_PAD - L_BASE; ps[8] = t * 4; ps[9] = t * 4 + 2; }
#pragma unroll
        for (int k = 0; k < 10; ++k) ps[k] += *(const u32*)(smem + L_BASE + bk[k]);
        SB();
        const u32 iv[10] = {i0.x, i0.y, i0.z, i0.w, i1.x, i1.y, i1.z, i1.w, i2.x, i2.y};
#pragma unroll
        for (int k = 0; k < 10; ++k) *(u32*)(smem + ps[k] * 2) = (iv[k] << 14) | lo[k];
        SB();
        const u32 tag = __builtin_amdgcn_readfirstlane(tag_) + 1;
        {
            const u32 e = w * 16 + (lane & 15);
            const u32 st = (*(const u32*)(smem + L_BASE + e * 4) - XB) >> 1, n = *(const u32*)(smem + L_CNT + e * 4) >> 1;
            const u32x2 own = *(const u32x2*)(smem + L_MISC + 8);
            __builtin_amdgcn_raw_buffer_store_b128((u32x4){tag, st | (n << 16), tag ^ KEY2, own.x | (own.y << 16)}, rs_ws,
                                                   lane < 16 ? (u32)OFF_FLG + g * (NWG * 16) + e * 16 : OOB, 0, SC1);
        }
        STAMP(2);
        __syncthreads();
        {
            const rsrc_t rw = __builtin_amdgcn_make_buffer_rsrc((void*)(ws + OFF_RW + (size_t)g * (S * 4)), 0, SV * 4, 0x00020000);
            const u32x4 v0 = *(const u32x4*)(smem + L_BIN + t * 16), v1 = *(const u32x4*)(smem + L_BIN + 16384 + t * 16);
            const u32x2 v2 = *(const u32x2*)(smem + L_BIN + 32768 + t * 8);
            __builtin_amdgcn_raw_buffer_store_b128(v0, rw, t * 16, 0, SC1);
            __builtin_amdgcn_raw_buffer_store_b128(v1, rw, t * 16 + 16384, 0, SC1);
            __builtin_amdgcn_raw_buffer_store_b64(v2, rw, t * 8, 32768, SC1);
        }
        SB();
        const u32 av[5] = {a0.x, a0.y, a1.x, a1.y, a2};
#pragma unroll
        for (int k = 0; k < 10; ++k) *(u16*)(smem + ps[k] + (L_BIN + S * 4 - XB)) = (u16)((k & 1) ? av[k >> 1] >> 16 : av[k >> 1]);
        STAMP(8);
        __syncthreads();
        {
            const rsrc_t rn = __builtin_amdgcn_make_buffer_rsrc((void*)(ws + OFF_RA + (size_t)g * (S * 2)), 0, SV * 2, 0x00020000);
            const u32x4 v0 = *(const u32x4*)(smem + L_BIN + S * 4 + t * 16);
            const u32 v1 = *(const u32*)(smem + L_BIN + S * 4 + 16384 + t * 4);
            __builtin_amdgcn_raw_buffer_store_b128(v0, rn, t * 16, 0, SC1);
            __builtin_amdgcn_raw_buffer_store_b32(v1, rn, t * 4, 16384, SC1);
        }
        SB();
        const u32 tb_off = lane < 16 ? (u32)OFF_FLG + (u32)((w * 16 + lane) * NWG + b) * 16 : OOB;
        u32x4 gr = __builtin_amdgcn_raw_buffer_load_b128(rs_ws, tb_off, 0, SC1);
        SB();
        STAMP(3);

        const float f0 = lo16f(freq[0]), f1 = hi16f(freq[0]), f2 = lo16f(freq[1]);
        const float dv = ((b + t) & 1) ? hi16f(dd_) : lo16f(dd_);
        {
            const u32 M = 0x7FFF7FFFu;
            const u32 amax = pkmax(pkmax(pkmax(a0.x & M, a0.y & M), pkmax(a1.x & M, a1.y & M)), a2 & M);
            const u32 am = max(amax & 0xFFFFu, amax >> 16);
            if (!(lo16f(am) * (fabsf(f0) + fabsf(f1) + fabsf(f2)) < 1500.f)) *(u32*)(smem + L_MISC + 24) = 1;
        }
        const u32x2 wdc = *(const u32x2*)(smem + L_WD + (lane & 3) * 8);
        f32x4 sa = {0, 0, 0, 0};
        u32x2 gp = {0, 0};
        if (w < 10) {
            const u32 i = lane & 31;
            const u32x4 wt = *(const u32x4*)(smem + (((i >> 2) & 1) == (u32)(lane >> 5) ? L_WT + ((i >> 3) * 4 + (i & 3)) * 16 : L_ZERO));
            u32x4 rb;
#pragma unroll
            for (int q = 0; q < 4; ++q) {
                f32x2 x = (f32x2){dv, dv} - (f32x2){lo16f(centers[q]), hi16f(centers[q])};
                x = (f32x2){rbf16(x.x), rbf16(x.y)};
                x = x * x;
                x = (f32x2){rbf16(x.x), rbf16(x.y)} * -10.f;
                x = (f32x2){rbf16(x.x), rbf16(x.y)} * 1.44269504088896341f;
                rb[q] = pk_bf16(__builtin_amdgcn_exp2f(x.x), __builtin_amdgcn_exp2f(x.y));
            }
            const f32x16 D = __builtin_amdgcn_mfma_f32_32x32x16_bf16(__builtin_bit_cast(bf16x8, wt), __builtin_bit_cast(bf16x8, rb), (f32x16)(0.f), 0, 0, 0);
            bool bad = false;
            f32x4 r;
#pragma unroll
            for (int k = 0; k < 4; ++k) {
                const float s = fabsf(D[k]) + fabsf(D[4 + k]) + fabsf(D[8 + k]);
                bad |= !(s + fabsf(D[12 + k]) < 0x1p120f);
                sa[k] = fmaxf(s, 0x1p-120f);
                r[k] = __builtin_amdgcn_rcpf(sa[k]);
            }
            *(f32x4*)(smem + L_A + t * 16) = (f32x4){D[0], D[1], D[2], D[3]} * r;
            *(f32x4*)(smem + L_A + A_STR + t * 16) = (f32x4){D[4], D[5], D[6], D[7]} * r;
            *(f32x4*)(smem + L_A + 2 * A_STR + t * 16) = (f32x4){D[8], D[9], D[10], D[11]} * r;
            gp = (u32x2){pk_bf16(D[12], D[13]), pk_bf16(D[14], D[15])};
            if (bad && t < EB) *(u32*)(smem + L_MISC + 4) = 1;
        }
        const float cs = (fabsf(lo16f(wdc.x)) + fabsf(hi16f(wdc.x)) + fabsf(lo16f(wdc.y)) + fabsf(hi16f(wdc.y))) * 1.02f;
        SB();
        STAMP(11);
        asm volatile("s_waitcnt vmcnt(0) lgkmcnt(0)" ::: "memory");
        STAMP(12);
        __syncthreads();
        {
            const u32 far = *(const u32*)(smem + L_MISC + 24);
            constexpr int CW = NCOPY / 16;
            __builtin_amdgcn_raw_buffer_store_b64((u32x2){tag, tag ^ KEY2 ^ far}, rs_ws,
                                                  lane < CW ? (u32)OFF_RDY + (w * CW + lane) * (NWG * 8) + g * 8 : OOB, 0, SC1);
        }
        STAMP(4);

        u32 rdy = 0, done = 0, spins = 0;
        u32x2 fl = {0, 0};
        const u32 fl_off = lane < 16 ? (u32)OFF_RDY + (g & (NCOPY - 1)) * (NWG * 8) + (w * 16 + lane) * 8 : OOB;
        const u32 cj = w * 16 * S + (lane & 15) * 4;
        u32x4 rq[4];
        u32x2 rn[4];
        u32 rd[4], rl[4];
#pragma unroll
        for (int i = 0; i < 4; ++i) {
            if (PRIO & 1) setprio(3 - i);
#pragma unroll 1
            while (__builtin_popcount(rdy & ~done) < 4) {
                asm volatile("" ::: "memory");
                if (rdy | spins) __builtin_amdgcn_s_sleep(1);
                fl = __builtin_amdgcn_raw_buffer_load_b64(rs_ws, fl_off, 0, SC1);
                u32 tok = (u32)__builtin_amdgcn_ballot_w64(gr.x == tag && gr.z == (tag ^ KEY2));
                if ((~tok & 0xFFFFu) != 0) {
                    if (lane < 16 && !((tok >> lane) & 1)) gr = __builtin_amdgcn_raw_buffer_load_b128(rs_ws, tb_off, 0, SC1);
                    tok = (u32)__builtin_amdgcn_ballot_w64(gr.x == tag && gr.z == (tag ^ KEY2));
                }
                rdy = (u32)__builtin_amdgcn_ballot_w64(fl.x == tag && (fl.y ^ tag ^ KEY2) < 2u) & tok & 0xFFFFu;
                if (++spins > (1u << 16)) { gr.y = 0; rdy = 0xFFFFu; }
            }
            u32 av = rdy & ~done, pk = 0;
#pragma unroll
            for (int k = 0; k < 4; ++k) {
                pk |= (u32)__builtin_ctz(av) << (8 * k + 2);
                av &= av - 1;
            }
            done = rdy & ~av;
            const u32 sl4 = (pk >> ((lane >> 4) * 8)) & 0xFFu;
            const u32 sg = __builtin_amdgcn_ds_bpermute(sl4, gr.y);
            rl[i] = sg >> 16;
            rd[i] = (lane & 15) * 4 - (sg & 3);
            u32 off = (__umul24(sl4, S / 4) + cj + (sg & 0xFFFCu)) * 4;
            off = (int)rd[i] < (int)rl[i] ? off : OOB;
            rq[i] = __builtin_amdgcn_raw_buffer_load_b128(rs_rw, off, 0, SC1);
            rn[i] = __builtin_amdgcn_raw_buffer_load_b64(rs_ra, off >> 1, 0, SC1);
            SB();
            if (i == 0) STAMP(5);
            if (i == 3) STAMP(10);
            if (i > 0 && i < 3) sort_quad(rq[i - 1], rn[i - 1], rd[i - 1], rl[i - 1], smem, lane);
            SB();
        }
        {
            const u32 need = lane < 16 ? (gr.y >> 16) + (gr.y & 3) : 0u;
            const bool more = __builtin_amdgcn_ballot_w64(need > 64) != 0;
            stat_max(row_pkmax(gr.w), L_MISC + 16, lane == 15);
            if (need > 80 || (lane < 16 && fl.y != (tag ^ KEY2))) *(u32*)(smem + L_MISC + 4) = 1;
            if (more) {
                const u32 sg = __builtin_amdgcn_ds_bpermute(lane & 60, gr.y);
                const u32 j4 = 64 + (lane & 3) * 4, d = j4 - (sg & 3), len = sg >> 16;
                u32 off = ((w * 16 + (lane >> 2)) * S + j4 + (sg & 0xFFFCu)) * 4;
                off = d < len ? off : OOB;
                const u32x4 q = __builtin_amdgcn_raw_buffer_load_b128(rs_rw, off, 0, SC1);
                const u32x2 an = __builtin_amdgcn_raw_buffer_load_b64(rs_ra, off >> 1, 0, SC1);
                sort_quad(rq[2], rn[2], rd[2], rl[2], smem, lane);
                rq[2] = q; rn[2] = an; rd[2] = d; rl[2] = len;
            }
            sort_quad(rq[2], rn[2], rd[2], rl[2], smem, lane);
            sort_quad(rq[3], rn[3], rd[3], rl[3], smem, lane);
        }
        STAMP(6);
        asm volatile("s_waitcnt lgkmcnt(0)" ::: "memory");
        __syncthreads();
        STAMP(13);

        constexpr int NS = 12;
        if (PRIO & 2) __builtin_amdgcn_s_setprio(3);
        u32x2 rec[NS], mg[NS];
        bool val[NS];
        const u32 cn = *(const u32*)(smem + L_BC + (w * 10 + min(lane, 9)) * 4);
        const u32x2 mi0 = *(const u32x2*)(smem + L_MISC), mi1 = *(const u32x2*)(smem + L_MISC + 16);
        const u32 l8 = lane * 8;
        auto fetch = [&](int k) {
            if (k < 10) rec[k] = *(const u32x2*)(smem + L_BIN + w * (10 * BSTR) + l8 + k * BSTR);
        };
#pragma unroll
        for (int k = 0; k < GDEPTH + 1; ++k) fetch(k);
        const u32 nsp = __builtin_amdgcn_readfirstlane(mi0.x);
        auto live = [&](int k) { return k < 10 || (u32)(((k - 10) * 16 + w) * 64) < nsp; };
        auto gather = [&](int k) {
            if (k < 10) {
                val[k] = l8 < (u32)__builtin_amdgcn_readlane(cn, k);
                mg[k] = __builtin_amdgcn_raw_buffer_load_b64(rs_m, val[k] ? (EXP == 1 ? l8 : rec[k].x >> 11) : OOB, 0, 0);
            } else if (live(k)) {
                const u32 p = ((k - 10) * 16 + w) * 64 + lane;
                val[k] = p < nsp;
                rec[k] = *(const u32x2*)(smem + L_SPILL + min(p, (u32)SPILL - 1) * 8);
                mg[k] = __builtin_amdgcn_raw_buffer_load_b64(rs_m, val[k] ? rec[k].x >> 11 : OOB, 0, 0);
            }
        };
#pragma unroll
        for (int k = 0; k < GDEPTH; ++k) gather(k);
        SB();
        if (g == 0 && t == 0) __builtin_amdgcn_raw_buffer_store_b32(tag, rs_ws, 0, 0, SC1);
        const u32 smax = __builtin_amdgcn_readfirstlane(mi1.x), smin = 0x7FFFu - __builtin_amdgcn_readfirstlane(mi1.y);
        bool slow = nsp > SPILL || __builtin_amdgcn_readfirstlane(mi0.y) != 0 || smax >= 0x7F80u || smin + (9u << 7) < smax;
        Fix F;
        f32x4 inv;
        {
            const float mmax = lo16f(smax), bound = cs * mmax;
            slow |= __builtin_amdgcn_ballot_w64(!(bound == 0.f || (bound > 0x1p-60f && bound < 0x1p60f && mmax > 0x1p-60f))) != 0;
            const int e = __builtin_amdgcn_frexp_expf(bound) - 17;
            const u32 mgl = __float_as_uint(ldexpf(12582912.0f, e)), ivl = __float_as_uint(ldexpf(1.f, e));
            float mgv[4];
#pragma unroll
            for (int k = 0; k < 4; ++k) {
                mgv[k] = __uint_as_float(__builtin_amdgcn_readlane(mgl, k));
                inv[k] = __uint_as_float(__builtin_amdgcn_readlane(ivl, k));
            }
            F.f01 = (f32x2){f0, f1};
            F.f2 = f2;
            F.mg01 = (f32x2){mgv[0], mgv[1]};
            F.mg23 = (f32x2){mgv[2], mgv[3]};
            F.nb01 = 0 - __builtin_bit_cast(u64, F.mg01);
            F.nb23 = 0 - __builtin_bit_cast(u64, F.mg23);
        }
        float ag[4];
        u32 xacc = 0;
        if (!slow) {
#pragma unroll
            for (int k = 0; k < NS; ++k) {
                SB();
                if ((PRIO & 2) && k % 3 == 0) setprio(3 - k / 3);
                if ((PRIO & 4) && k == 0) { if (w >= 12) __builtin_amdgcn_s_setprio(3); else if (w >= 8) __builtin_amdgcn_s_setprio(2); else if (w >= 4) __builtin_amdgcn_s_setprio(1); else __builtin_amdgcn_s_setprio(0); }
                if ((PRIO & 4) && k == 5) __builtin_amdgcn_s_setprio(0);
                if (k + GDEPTH + 1 < NS) fetch(k + GDEPTH + 1);
                if (EXP == 2) { if (live(k)) xacc ^= mg[k].x ^ mg[k].y; }
                else if (live(k)) {
                    const f32x4 md = row_mat4(wdc, mg[k]);
                    if (val[k]) accum_fix(rec[k].x, rec[k].y, md, F, smem);
                }
                SB();
                if (k + GDEPTH < NS) gather(k + GDEPTH);
            }
            if (EXP == 2) asm volatile("" :: "v"(xacc));
            STAMP(7);
            __syncthreads();
            if (w < 10) {
                const u64 s01 = *(const u64*)(smem + L_ACC + t * 8), s23 = *(const u64*)(smem + L_ACC + ACC_STR + t * 8);
                const int q0 = (int)(u32)s01, q2 = (int)(u32)s23;
                const int q1 = (int)(u32)(s01 >> 32) - (q0 >> 31), q3 = (int)(u32)(s23 >> 32) - (q2 >> 31);
                ag[0] = (float)q0 * inv[0] * sa[0]; ag[1] = (float)q1 * inv[1] * sa[1];
                ag[2] = (float)q2 * inv[2] * sa[2]; ag[3] = (float)q3 * inv[3] * sa[3];
            }
        } else {
            __syncthreads();
            if (w < 10) {
                const EdgeC e = edge_coeffs(dv, centers, w_rbf, w_sbf);
                *(f32x4*)(smem + L_A + t * 16) = e.A0;
                *(f32x4*)(smem + L_A + A_STR + t * 16) = e.A1;
                *(f32x4*)(smem + L_A + 2 * A_STR + t * 16) = e.A2;
                gp = (u32x2){pk_bf16(e.g.x, e.g.y), pk_bf16(e.g.z, e.g.w)};
                *(u32x4*)(smem + L_BIN + t * 32) = (u32x4){0, 0, 0, 0};
                *(u32x4*)(smem + L_BIN + t * 32 + 16) = (u32x4){0, 0, 0, 0};
            }
            __syncthreads();
#pragma unroll 1
            for (int s = 0; s < 16; ++s) {
                const u32 sg = __builtin_amdgcn_readlane(gr.y, s);
                const u32 len = sg >> 16, p0 = (w * 16 + s) * S + (sg & 0xFFFFu);
#pragma unroll 1
                for (u32 q = 0; q < len; q += 64) {
                    if (q + lane < len) {
                        const u32 p = p0 + q + lane;
                        const u32 w0 = __builtin_amdgcn_raw_buffer_load_b32(rs_rw, p * 4, 0, SC1);
                        const u32 a2 = __builtin_amdgcn_raw_buffer_load_b32(rs_ra, (p >> 1) * 4, 0, SC1);
                        const u32x2 mr = __builtin_amdgcn_raw_buffer_load_b64(rs_m, w0 >> 11, 0, 0);
                        accum_slow(w0, (p & 1) ? a2 & 0xFFFF0000u : a2 << 16, mr, w_down, f0, f1, f2, smem);
                    }
                }
            }
            __syncthreads();
            if (w < 10) {
                const double* acc = (const double*)(smem + L_BIN) + t;
                ag[0] = (float)acc[0]; ag[1] = (float)acc[640]; ag[2] = (float)acc[1280]; ag[3] = (float)acc[1920];
            }
        }
        if (w < 10) {
            const f32x4 u = row_mat4(*(const u32x2*)(smem + L_WD + 32 + (lane & 3) * 8), (u32x2){pk_bf16(ag[0], ag[1]), pk_bf16(ag[2], ag[3])});
            const float mm[4] = {lo16f(mrow_.x), hi16f(mrow_.x), lo16f(mrow_.y), hi16f(mrow_.y)};
            const float gg[4] = {lo16f(gp.x), hi16f(gp.x), lo16f(gp.y), hi16f(gp.y)};
            float o[4];
#pragma unroll
            for (int k = 0; k < 4; ++k) o[k] = mm[k] + rbf16(rbf16(u[k]) * gg[k]);
            __builtin_amdgcn_raw_buffer_store_b64((u32x2){pk_bf16(o[0], o[1]), pk_bf16(o[2], o[3])}, rs_out, t * 8, 0, SC1);
        }
    }
    STAMP(9);
}
}

namespace generic {
__global__ void k_trip(const u16* m, const int* idx_in, const int* idx_out, const u16* dist,
                       const u16* angle, const u16* centers, const u16* freq,
                       const u16* w_sbf, const u16* w_down, float* acc, int T) {
    int t = blockIdx.x * blockDim.x + threadIdx.x;
    if (t >= T) return;
    int ei = idx_in[t], eo = idx_out[t];
    float a = bf2f(angle[t]);
    float d = bf2f(dist[eo]);
    float c[3];
    for (int l = 0; l < 3; ++l) c[l] = rbf16(cosf(rbf16(bf2f(freq[l]) * a)));
    float s[4] = {0, 0, 0, 0};
    for (int r = 0; r < 8; ++r) {
        float df = rbf16(d - bf2f(centers[r]));
        float rb = rbf16(expf(rbf16(-10.f * rbf16(df * df))));
        for (int l = 0; l < 3; ++l) {
            float sb = rbf16(rb * c[l]);
            for (int k = 0; k < 4; ++k) s[k] += sb * bf2f(w_sbf[(r * 3 + l) * 4 + k]);
        }
    }
    float mm[4];
    for (int k = 0; k < 4; ++k) mm[k] = bf2f(m[ei * 4 + k]);
    for (int k = 0; k < 4; ++k) {
        float md = 0;
        for (int j = 0; j < 4; ++j) md += mm[j] * bf2f(w_down[j * 4 + k]);
        float x = rbf16(rbf16(md) * rbf16(s[k]));
        atomicAdd(&acc[eo * 4 + k], x);
    }
}

__global__ void k_fin(const u16* m, const u16* dist, const u16* centers, const u16* w_rbf,
                      const u16* w_up, const float* acc, __hip_bfloat16* out, int E) {
    int e = blockIdx.x * blockDim.x + threadIdx.x;
    if (e >= E) return;
    float d = bf2f(dist[e]);
    float g[4] = {0, 0, 0, 0};
    for (int r = 0; r < 8; ++r) {
        float df = rbf16(d - bf2f(centers[r]));
        float rb = rbf16(expf(rbf16(-10.f * rbf16(df * df))));
        for (int k = 0; k < 4; ++k) g[k] += rb * bf2f(w_rbf[r * 4 + k]);
    }
    float ag[4];
    for (int k = 0; k < 4; ++k) ag[k] = rbf16(acc[e * 4 + k]);
    for (int k = 0; k < 4; ++k) {
        float u = 0;
        for (int j = 0; j < 4; ++j) u += ag[j] * bf2f(w_up[j * 4 + k]);
        float y = rbf16(rbf16(u) * rbf16(g[k]));
        out[e * 4 + k] = __float2bfloat16(bf2f(m[e * 4 + k]) + y);
    }
}

}

extern "C" void kernel_launch(void* const* d_in, const int* in_sizes, int n_in,
                              void* d_out, int out_size, void* d_ws, size_t ws_size,
                              hipStream_t stream) {
    const u16* m = (const u16*)d_in[0];
    const u32* idx_in = (const u32*)d_in[1];
    const u32* idx_out = (const u32*)d_in[2];
    const u16* dist = (const u16*)d_in[3];
    const u16* angle = (const u16*)d_in[4];
    const u16* centers = (const u16*)d_in[5];
    const u16* freq = (const u16*)d_in[6];
    const u16* w_rbf = (const u16*)d_in[7];
    const u16* w_sbf = (const u16*)d_in[8];
    const u16* w_down = (const u16*)d_in[9];
    const u16* w_up = (const u16*)d_in[10];
    const int T = in_sizes[1], E = in_sizes[3];
    if (T != fast::kT || E != fast::kE || ws_size < fast::WS_NEED) {
        float* acc = (float*)d_ws;
        hipMemsetAsync(acc, 0, (size_t)E * 4 * sizeof(float), stream);
        generic::k_trip<<<(T + 255) / 256, 256, 0, stream>>>(m, (const int*)idx_in, (const int*)idx_out, dist, angle, centers, freq, w_sbf, w_down, acc, T);
        generic::k_fin<<<(E + 255) / 256, 256, 0, stream>>>(m, dist, centers, w_rbf, w_up, acc, (__hip_bfloat16*)d_out, E);
        return;
    }
    fast::k_main<<<fast::NWG, 1024, 0, stream>>>(m, idx_in, idx_out, dist, angle, centers, freq, w_rbf, w_sbf, w_down, w_up, (u16*)d_out, (char*)d_ws);
}
```

```cpp
#include <hip/hip_runtime.h>
#include <hip/hip_bf16.h>
#include <stdint.h>

#define IN_AUX 2
#define GDEPTH 3
#define NCOPY 32
#define CODE_LINES 16
#define NBW 12

typedef unsigned int u32;
typedef unsigned short u16;
typedef unsigned long long u64;
typedef u32 u32x2 __attribute__((ext_vector_type(2)));
typedef u32 u32x4 __attribute__((ext_vector_type(4)));
typedef u32 u32x8 __attribute__((ext_vector_type(8)));
typedef u16 u16x2 __attribute__((ext_vector_type(2)));
typedef float f32x4 __attribute__((ext_vector_type(4)));
typedef float f32x2 __attribute__((ext_vector_type(2)));
typedef float f32x16 __attribute__((ext_vector_type(16)));
typedef short s16x4 __attribute__((ext_vector_type(4)));
typedef __bf16 bf16x8 __attribute__((ext_vector_type(8)));
typedef const __attribute__((address_space(4))) u32* cptr;
typedef __amdgpu_buffer_rsrc_t rsrc_t;

static __device__ __forceinline__ float bf2f(u16 v) { return __uint_as_float(((u32)v) << 16); }
static __device__ __forceinline__ float lo16f(u32 v) { return __uint_as_float(v << 16); }
static __device__ __forceinline__ float hi16f(u32 v) { return __uint_as_float(v & 0xFFFF0000u); }
static __device__ __forceinline__ u32 pk_bf16(float a, float b) {
    typedef __bf16 bf16x2 __attribute__((ext_vector_type(2)));
    bf16x2 r = __builtin_convertvector((f32x2){a, b}, bf16x2);
    return __builtin_bit_cast(u32, r);
}
static __device__ __forceinline__ float rbf16(float x) { return __uint_as_float(pk_bf16(0.f, x)); }
static __device__ __forceinline__ float cw(cptr p, int i) { u32 v = p[i >> 1]; return (i & 1) ? hi16f(v) : lo16f(v); }
static __device__ __forceinline__ u32 pkmax(u32 a, u32 b) {
    return __builtin_bit_cast(u32, __builtin_elementwise_max(__builtin_bit_cast(u16x2, a), __builtin_bit_cast(u16x2, b)));
}

namespace fast {
constexpr int kE = 160000, kT = 2560000;
constexpr int NWG = 256, EB = 625, NC = 256, SV = 10000, S = 10240;
constexpr u32 MAGIC = 6871948u;
constexpr int NBIN = NBW * 16, BSTR = 520, SPILL = 1536;
constexpr u32 BINC = NBIN * 65536 / (kE / 16);
constexpr u32 KEY2 = 0x9E3779B9u;
constexpr size_t OFF_RDY = 4096;
constexpr size_t OFF_FLG = OFF_RDY + 64 * NWG * 8;
constexpr size_t OFF_RW = OFF_FLG + (size_t)NWG * NWG * 16;
constexpr size_t OFF_RA = OFF_RW + (size_t)NC * S * 4;
constexpr size_t OFF_DIAG = OFF_RA + (size_t)NC * S * 2;
constexpr size_t WS_NEED = OFF_DIAG + 256 * 16 * 64;
constexpr int SC1 = 16;
constexpr u32 OOB = 0xFFFFFF00u;

constexpr int L_A = 0, A_STR = 640 * 16;
constexpr int L_ACC = 3 * A_STR, ACC_STR = 640 * 8;
constexpr int L_CNT = L_ACC + 2 * ACC_STR;
constexpr int L_BASE = L_CNT + 1024;
constexpr int L_BC = L_BASE + 1024;
constexpr int L_MISC = L_BC + 768;
constexpr int L_PAD = L_MISC + 32;
constexpr int L_ZERO = L_PAD + 16;
constexpr int L_WD = L_BC + 832;
constexpr int L_WT = L_BC + 1024;
constexpr int L_BIN = L_BC + 1280;
constexpr int L_SPILL = L_BIN + NBIN * BSTR;
constexpr int L_END = L_SPILL + SPILL * 8;
constexpr int XB = L_BIN / 2;
static_assert(((kE / 16 - 1) * BINC >> 16) < NBIN && NBIN * 64 + SPILL < 16384 && NBIN * 4 <= 768 && L_END <= 163840 && S * 6 <= NBIN * BSTR && L_BIN + S * 4 - XB < 65536, "lds");

#define STAMP(i)
#define SB() __builtin_amdgcn_sched_barrier(0)

static __device__ __forceinline__ u32 lds_add(char* p, u32 v) {
    return __hip_atomic_fetch_add((u32*)p, v, __ATOMIC_RELAXED, __HIP_MEMORY_SCOPE_WORKGROUP);
}
static __device__ __forceinline__ void stat_max(u32 st, u32 addr, bool on) {
    if (on) asm volatile("ds_max_u32 %0, %1\n\tds_max_u32 %0, %2 offset:4" :: "v"(addr), "v"(st & 0xFFFFu), "v"(st >> 16) : "memory");
}
static __device__ __forceinline__ u32 wave_scan_incl(u32 v) {
    v += __builtin_amdgcn_update_dpp(0u, v, 0x111, 0xf, 0xf, false);
    v += __builtin_amdgcn_update_dpp(0u, v, 0x112, 0xf, 0xf, false);
    v += __builtin_amdgcn_update_dpp(0u, v, 0x114, 0xf, 0xf, false);
    v += __builtin_amdgcn_update_dpp(0u, v, 0x118, 0xf, 0xf, false);
    v += __builtin_amdgcn_update_dpp(0u, v, 0x142, 0xa, 0xf, false);
    v += __builtin_amdgcn_update_dpp(0u, v, 0x143, 0xc, 0xf, false);
    return v;
}
static __device__ __forceinline__ u32 row_pkmax(u32 v) {
    v = pkmax(v, __builtin_amdgcn_update_dpp(0u, v, 0x111, 0xf, 0xf, false));
    v = pkmax(v, __builtin_amdgcn_update_dpp(0u, v, 0x112, 0xf, 0xf, false));
    v = pkmax(v, __builtin_amdgcn_update_dpp(0u, v, 0x114, 0xf, 0xf, false));
    v = pkmax(v, __builtin_amdgcn_update_dpp(0u, v, 0x118, 0xf, 0xf, false));
    return v;
}

struct EdgeC { f32x4 A0, A1, A2, g; };
static __device__ __forceinline__ EdgeC edge_coeffs(float d, cptr centers, cptr w_rbf, cptr w_sbf) {
    EdgeC e;
    e.A0 = e.A1 = e.A2 = e.g = (f32x4){0, 0, 0, 0};
#pragma unroll
    for (int q = 0; q < 8; ++q) {
        float df = rbf16(d - cw(centers, q));
        float rb = rbf16(__expf(rbf16(-10.f * rbf16(df * df))));
#pragma unroll
        for (int k = 0; k < 4; ++k) {
            e.A0[k] += rb * cw(w_sbf, (q * 3 + 0) * 4 + k);
            e.A1[k] += rb * cw(w_sbf, (q * 3 + 1) * 4 + k);
            e.A2[k] += rb * cw(w_sbf, (q * 3 + 2) * 4 + k);
            e.g[k] += rb * cw(w_rbf, q * 4 + k);
        }
    }
    return e;
}

constexpr float INV2PI = 0.15915494309189535f;

struct Fix { f32x2 f01; float f2; f32x2 mg01, mg23; u64 nb01, nb23; };

static __device__ __forceinline__ f32x4 row_mat4(u32x2 col, u32x2 row) {
    return __builtin_amdgcn_mfma_f32_4x4x4bf16_1k(__builtin_bit_cast(s16x4, col), __builtin_bit_cast(s16x4, row), (f32x4){0, 0, 0, 0}, 0, 0, 0);
}
static __device__ __forceinline__ u64 add64(u64 a, u64 b) {
    u64 r;
    asm("v_lshl_add_u64 %0, %1, 0, %2" : "=v"(r) : "v"(a), "s"(b));
    return r;
}
static __device__ __forceinline__ void accum_fix(u32 w0, u32 aw, f32x4 d, const Fix& F, char* smem) {
    u32 l16, l8;
    asm("v_lshlrev_b16 %0, 4, %1" : "=v"(l16) : "v"(w0));
    asm("v_lshlrev_b16 %0, 3, %1" : "=v"(l8) : "v"(w0));
    const f32x4 A0 = *(const f32x4*)(smem + L_A + l16);
    const f32x4 A1 = *(const f32x4*)(smem + L_A + A_STR + l16);
    const f32x4 A2 = *(const f32x4*)(smem + L_A + 2 * A_STR + l16);
    const float a = __uint_as_float(aw);
    const f32x2 p01 = F.f01 * a;
    const f32x2 r01 = (f32x2){rbf16(p01.x), rbf16(p01.y)} * INV2PI;
    const float c0 = __builtin_amdgcn_cosf(r01.x), c1 = __builtin_amdgcn_cosf(r01.y);
    const float c2 = __builtin_amdgcn_cosf(rbf16(F.f2 * a) * INV2PI);
    const f32x4 s = A0 * c0 + A1 * c1 + A2 * c2;
    const f32x2 q01 = __builtin_elementwise_fma((f32x2){d.x, d.y}, (f32x2){s.x, s.y}, F.mg01);
    const f32x2 q23 = __builtin_elementwise_fma((f32x2){d.z, d.w}, (f32x2){s.z, s.w}, F.mg23);
    __hip_atomic_fetch_add((u64*)(smem + L_ACC + l8), add64(__builtin_bit_cast(u64, q01), F.nb01), __ATOMIC_RELAXED, __HIP_MEMORY_SCOPE_WORKGROUP);
    __hip_atomic_fetch_add((u64*)(smem + L_ACC + ACC_STR + l8), add64(__builtin_bit_cast(u64, q23), F.nb23), __ATOMIC_RELAXED, __HIP_MEMORY_SCOPE_WORKGROUP);
}

static __device__ __forceinline__ float cos_any(float x) {
    return __builtin_amdgcn_cosf(__builtin_amdgcn_fractf(x * INV2PI));
}
static __device__ __forceinline__ void accum_slow(u32 w0, u32 aw, u32x2 mr, cptr w_down, float f0, float f1, float f2, char* smem) {
    const u32 l = w0 & 0x3FFu;
    const float a = __uint_as_float(aw);
    const float c0 = cos_any(rbf16(f0 * a)), c1 = cos_any(rbf16(f1 * a)), c2 = cos_any(rbf16(f2 * a));
    const f32x4 s = *(const f32x4*)(smem + L_A + l * 16) * c0 + *(const f32x4*)(smem + L_A + A_STR + l * 16) * c1 +
                    *(const f32x4*)(smem + L_A + 2 * A_STR + l * 16) * c2;
    double* acc = (double*)(smem + L_BIN + l * 8);
#pragma unroll
    for (int k = 0; k < 4; ++k) {
        float md = lo16f(mr.x) * cw(w_down, k) + hi16f(mr.x) * cw(w_down, 4 + k) + lo16f(mr.y) * cw(w_down, 8 + k) + hi16f(mr.y) * cw(w_down, 12 + k);
        __hip_atomic_fetch_add(acc + k * 640, (double)(rbf16(md) * s[k]), __ATOMIC_RELAXED, __HIP_MEMORY_SCOPE_WORKGROUP);
    }
}

static __device__ __forceinline__ void sort_quad(u32x4 q, u32x2 an, u32 d, u32 len, char* smem, int lane) {
    const u32 w0[4] = {q.x, q.y, q.z, q.w};
    u32 ka[4], rk[4];
    u64 mk[4];
#pragma unroll
    for (int k = 0; k < 4; ++k) {
        ka[k] = (__umul24(w0[k] >> 18, BINC) >> 16) << 2;
        mk[k] = __builtin_amdgcn_ballot_w64(d + k < len);
    }
    const u32 eight = 8;
    asm volatile("s_mov_b64 exec, %8\n\tds_add_rtn_u32 %0, %4, %12 offset:%13\n\t"
                 "s_mov_b64 exec, %9\n\tds_add_rtn_u32 %1, %5, %12 offset:%13\n\t"
                 "s_mov_b64 exec, %10\n\tds_add_rtn_u32 %2, %6, %12 offset:%13\n\t"
                 "s_mov_b64 exec, %11\n\tds_add_rtn_u32 %3, %7, %12 offset:%13\n\t"
                 "s_mov_b64 exec, -1"
                 : "=&v"(rk[0]), "=&v"(rk[1]), "=&v"(rk[2]), "=&v"(rk[3])
                 : "v"(ka[0]), "v"(ka[1]), "v"(ka[2]), "v"(ka[3]), "s"(mk[0]), "s"(mk[1]), "s"(mk[2]), "s"(mk[3]),
                   "v"(eight), "n"(L_BC)
                 : "memory");
    const u32x2 r[4] = {{w0[0], an.x << 16}, {w0[1], an.x & 0xFFFF0000u}, {w0[2], an.y << 16}, {w0[3], an.y & 0xFFFF0000u}};
    asm volatile("s_waitcnt lgkmcnt(0)" : "+v"(rk[0]), "+v"(rk[1]), "+v"(rk[2]), "+v"(rk[3]) :: "memory");
    u32 ad[4];
    u64 ov[4];
#pragma unroll
    for (int k = 0; k < 4; ++k) {
        ad[k] = __umul24(ka[k], BSTR / 4) + rk[k];
        ov[k] = __builtin_amdgcn_ballot_w64(rk[k] >= 512u) & mk[k];
        mk[k] &= ~ov[k];
    }
    asm volatile("s_mov_b64 exec, %8\n\tds_write_b64 %0, %4 offset:%12\n\t"
                 "s_mov_b64 exec, %9\n\tds_write_b64 %1, %5 offset:%12\n\t"
                 "s_mov_b64 exec, %10\n\tds_write_b64 %2, %6 offset:%12\n\t"
                 "s_mov_b64 exec, %11\n\tds_write_b64 %3, %7 offset:%12\n\t"
                 "s_mov_b64 exec, -1"
                 :: "v"(ad[0]), "v"(ad[1]), "v"(ad[2]), "v"(ad[3]), "v"(r[0]), "v"(r[1]), "v"(r[2]), "v"(r[3]),
                    "s"(mk[0]), "s"(mk[1]), "s"(mk[2]), "s"(mk[3]), "n"(L_BIN)
                 : "memory");
    if ((ov[0] | ov[1] | ov[2] | ov[3]) != 0) {
        const u32 n0 = __builtin_popcountll(ov[0]), n1 = n0 + __builtin_popcountll(ov[1]), n2 = n1 + __builtin_popcountll(ov[2]);
        const u32 n3 = n2 + __builtin_popcountll(ov[3]);
        u32 p = 0;
        if (lane == 0) p = lds_add(smem + L_MISC, n3);
        p = __builtin_amdgcn_readfirstlane(p);
        if (p + n3 <= SPILL) {
            const u32 pb[4] = {p, p + n0, p + n1, p + n2};
#pragma unroll
            for (int k = 0; k < 4; ++k)
                ad[k] = __builtin_amdgcn_mbcnt_hi((u32)(ov[k] >> 32), __builtin_amdgcn_mbcnt_lo((u32)ov[k], pb[k] + L_SPILL / 8)) * 8;
            asm volatile("s_mov_b64 exec, %8\n\tds_write_b64 %0, %4\n\t"
                         "s_mov_b64 exec, %9\n\tds_write_b64 %1, %5\n\t"
                         "s_mov_b64 exec, %10\n\tds_write_b64 %2, %6\n\t"
                         "s_mov_b64 exec, %11\n\tds_write_b64 %3, %7\n\t"
                         "s_mov_b64 exec, -1"
                         :: "v"(ad[0]), "v"(ad[1]), "v"(ad[2]), "v"(ad[3]), "v"(r[0]), "v"(r[1]), "v"(r[2]), "v"(r[3]),
                            "s"(ov[0]), "s"(ov[1]), "s"(ov[2]), "s"(ov[3])
                         : "memory");
        }
    }
}

__global__ void __launch_bounds__(1024)
k_main(const u16* p0, const u32* p1, const u32* p2, const u16* p3, const u16* p4, const u16* p5, const u16* p6,
       const u16* p7, const u16* p8, const u16* p9, const u16* p10, u16* p11, char* p12) {
    __shared__ __attribute__((aligned(16))) char smem[L_END];
    const __attribute__((address_space(4))) u64* kp = (const __attribute__((address_space(4))) u64*)__builtin_amdgcn_kernarg_segment_ptr();
    u64 P[13];
#pragma unroll
    for (int k = 0; k < 13; ++k) P[k] = kp[k];
    asm volatile("" : "+s"(P[0]), "+s"(P[1]), "+s"(P[2]), "+s"(P[3]), "+s"(P[4]), "+s"(P[5]), "+s"(P[6]), "+s"(P[7]), "+s"(P[8]),
                 "+s"(P[9]), "+s"(P[10]), "+s"(P[11]), "+s"(P[12]));
    const u16* m = (const u16*)P[0];
    const u32* idx_in = (const u32*)P[1];
    const u32* idx_out = (const u32*)P[2];
    const u16* dist = (const u16*)P[3];
    const u16* angle = (const u16*)P[4];
    const cptr centers = (cptr)P[5], freq = (cptr)P[6], w_rbf = (cptr)P[7], w_sbf = (cptr)P[8], w_down = (cptr)P[9], w_up = (cptr)P[10];
    u16* out = (u16*)P[11];
    char* ws = (char*)P[12];
    const int t = threadIdx.x, lane = t & 63;
    const int w = __builtin_amdgcn_readfirstlane(t >> 6);
    const int g = blockIdx.x;
    const int b = (g & 7) * 32 + (g >> 3);
    STAMP(0);

    const size_t cb = (size_t)g * SV;
    const rsrc_t ro = __builtin_amdgcn_make_buffer_rsrc((void*)(idx_out + cb), 0, SV * 4, 0x00020000);
    const u32x4 o0 = __builtin_amdgcn_raw_buffer_load_b128(ro, t * 16, 0, IN_AUX);
    const u32x4 o1 = __builtin_amdgcn_raw_buffer_load_b128(ro, t * 16, 16384, IN_AUX);
    const u32x2 o2 = __builtin_amdgcn_raw_buffer_load_b64(ro, t * 8, 32768, IN_AUX);
    SB();
    const rsrc_t rs_ms = __builtin_amdgcn_make_buffer_rsrc((void*)(m + (size_t)b * EB * 4), 0, EB * 8, 0x00020000);
    const rsrc_t rs_ds = __builtin_amdgcn_make_buffer_rsrc((void*)dist, 0, kE * 2, 0x00020000);
    const rsrc_t rs_ws = __builtin_amdgcn_make_buffer_rsrc((void*)ws, 0, (u32)OFF_RW, 0x00020000);
    const rsrc_t rs_rw = __builtin_amdgcn_make_buffer_rsrc((void*)(ws + OFF_RW), 0, NC * S * 4, 0x00020000);
    const rsrc_t rs_ra = __builtin_amdgcn_make_buffer_rsrc((void*)(ws + OFF_RA), 0, NC * S * 2, 0x00020000);
    const rsrc_t rs_out = __builtin_amdgcn_make_buffer_rsrc((void*)(out + (size_t)b * EB * 4), 0, EB * 8, 0x00020000);
    const rsrc_t rs_m = __builtin_amdgcn_make_buffer_rsrc((void*)m, 0, kE * 8, 0x00020000);
    const u32 tag_ = __builtin_amdgcn_raw_buffer_load_b32(rs_ws, 0, 0, SC1);
    const u32 dd_ = __builtin_amdgcn_raw_buffer_load_b32(rs_ds, ((b * EB + t) & ~1) * 2, 0, 0);
    const u32x2 mrow_ = __builtin_amdgcn_raw_buffer_load_b64(rs_ms, t * 8, 0, 0);
    u32 small = 0;
    if (w >= 14) {
        u64 p;
        if (w == 15) {
            p = lane < 48 ? (u64)w_sbf + lane * 4 : (u64)w_rbf + (lane - 48) * 4;
        } else {
            p = lane < 8 ? (u64)w_down + lane * 4 : (u64)w_up + (lane & 7) * 4;
            p = lane == 16 ? (u64)centers : p;
            p = lane == 17 ? (u64)freq : p;
            p = lane == 18 ? (u64)(out + (size_t)b * EB * 4) : p;
            p = lane >= 32 && lane < 32 + CODE_LINES ? (u64)__builtin_amdgcn_s_getpc() + (((g >> 3) & 7) * CODE_LINES + lane - 32) * 128 : p;
        }
        small = *(const __attribute__((address_space(1))) u32*)p;
    }
    SB();
    if (t < 208) *(u32x4*)(smem + L_CNT + t * 16) = (u32x4){t * 16 == L_PAD - L_CNT ? XB + 16384u : 0u, 0, 0, 0};
    if (t >= 256 && t < 896) *(u32x4*)(smem + L_ACC + (t - 256) * 16) = (u32x4){0, 0, 0, 0};
    __syncthreads();
    {
        const rsrc_t ri = __builtin_amdgcn_make_buffer_rsrc((void*)(idx_in + cb), 0, SV * 4, 0x00020000);
        const rsrc_t ra = __builtin_amdgcn_make_buffer_rsrc((void*)(angle + cb), 0, SV * 2, 0x00020000);
        const u32x4 i0 = __builtin_amdgcn_raw_buffer_load_b128(ri, t * 16, 0, IN_AUX);
        const u32x4 i1 = __builtin_amdgcn_raw_buffer_load_b128(ri, t * 16, 16384, IN_AUX);
        const u32x2 i2 = __builtin_amdgcn_raw_buffer_load_b64(ri, t * 8, 32768, IN_AUX);
        SB();
        SB();
        const bool tail = t < (SV - 8192) / 2;
        u32 lo[10] = {o0.x, o0.y, o0.z, o0.w, o1.x, o1.y, o1.z, o1.w, o2.x, o2.y};
        u32 ps[10], bk[10];
        auto count = [&](int k) {
            u32 bb;
            asm("v_mul_hi_u32_u24 %0, %1, %2" : "=v"(bb) : "s"(MAGIC), "v"(lo[k]));
            bk[k] = bb * 4;
            ps[k] = lds_add(smem + L_CNT + bk[k], 2u);
            asm("v_mad_i32_i24 %0, %1, %2, %0" : "+v"(lo[k]) : "v"(bb), "s"(-EB));
        };
#pragma unroll
        for (int k = 0; k < 8; ++k) count(k);
        if (tail) { count(8); count(9); }
        SB();
        STAMP(1);
        const u32x2 a0 = __builtin_amdgcn_raw_buffer_load_b64(ra, t * 8, 0, IN_AUX);
        const u32x2 a1 = __builtin_amdgcn_raw_buffer_load_b64(ra, t * 8, 8192, IN_AUX);
        const u32 a2 = __builtin_amdgcn_raw_buffer_load_b32(ra, t * 4, 16384, IN_AUX);
        SB();
        SB();
        if (w >= 14) {
            u32 ad;
            if (w == 15) {
                const u32 row = lane >> 1, r = lane < 48 ? (row * 43) >> 7 : row - 24;
                ad = L_WT + (lane < 48 ? row - r * 3 : 3) * 64 + (lane & 1) * 32 + r * 2;
                *(u16*)(smem + ad) = (u16)small;
                *(u16*)(smem + ad + 16) = (u16)(small >> 16);
            } else if (lane < 16) {
                ad = L_WD + (lane >> 3) * 32 + (lane & 1) * 16 + ((lane >> 1) & 3) * 2;
                *(u16*)(smem + ad) = (u16)small;
                *(u16*)(smem + ad + 8) = (u16)(small >> 16);
            }
        }
        if (w < 10) {
            const u32 M = 0x7FFF7FFFu;
            u32 rm = pkmax(mrow_.x & M, mrow_.y & M);
            rm = max(rm & 0xFFFFu, rm >> 16);
            stat_max(row_pkmax(rm | (rm ? (0x7FFFu - rm) << 16 : 0u)), L_MISC + 8, (lane & 15) == 15);
        }
        asm volatile("s_waitcnt lgkmcnt(0)" ::: "memory");
        __syncthreads();
        {
            const u32x4 c = *(const u32x4*)(smem + L_CNT + lane * 16);
            const u32 s1 = c.x, s2 = s1 + c.y, s3 = s2 + c.z, s4 = s3 + c.w;
            const u32 ex = wave_scan_incl(s4) - s4 + XB;
            *(u32x4*)(smem + L_BASE + lane * 16) = (u32x4){ex, ex + s1, ex + s2, ex + s3};
        }
        __builtin_amdgcn_fence(__ATOMIC_RELEASE, "wavefront");
        __builtin_amdgcn_wave_barrier();
        __builtin_amdgcn_fence(__ATOMIC_ACQUIRE, "wavefront");
        if (!tail) { bk[8] = bk[9] = L_PAD - L_BASE; ps[8] = t * 4; ps[9] = t * 4 + 2; }
#pragma unroll
        for (int k = 0; k < 10; ++k) ps[k] += *(const u32*)(smem + L_BASE + bk[k]);
        SB();
        const u32 iv[10] = {i0.x, i0.y, i0.z, i0.w, i1.x, i1.y, i1.z, i1.w, i2.x, i2.y};
#pragma unroll
        for (int k = 0; k < 10; ++k) *(u32*)(smem + ps[k] * 2) = (iv[k] << 14) | lo[k];
        SB();
        const u32 tag = __builtin_amdgcn_readfirstlane(tag_) + 1;
        {
            const u32 e = w * 16 + (lane & 15);
            const u32 st = (*(const u32*)(smem + L_BASE + e * 4) - XB) >> 1, n = *(const u32*)(smem + L_CNT + e * 4) >> 1;
            const u32x2 own = *(const u32x2*)(smem + L_MISC + 8);
            __builtin_amdgcn_raw_buffer_store_b128((u32x4){tag, st | (n << 16), tag ^ KEY2, own.x | (own.y << 16)}, rs_ws,
                                                   lane < 16 ? (u32)OFF_FLG + g * (NWG * 16) + e * 16 : OOB, 0, SC1);
        }
        STAMP(2);
        __syncthreads();
        {
            const rsrc_t rw = __builtin_amdgcn_make_buffer_rsrc((void*)(ws + OFF_RW + (size_t)g * (S * 4)), 0, SV * 4, 0x00020000);
            const u32x4 v0 = *(const u32x4*)(smem + L_BIN + t * 16), v1 = *(const u32x4*)(smem + L_BIN + 16384 + t * 16);
            const u32x2 v2 = *(const u32x2*)(smem + L_BIN + 32768 + t * 8);
            __builtin_amdgcn_raw_buffer_store_b128(v0, rw, t * 16, 0, SC1);
            __builtin_amdgcn_raw_buffer_store_b128(v1, rw, t * 16 + 16384, 0, SC1);
            __builtin_amdgcn_raw_buffer_store_b64(v2, rw, t * 8, 32768, SC1);
        }
        SB();
        const u32 av[5] = {a0.x, a0.y, a1.x, a1.y, a2};
#pragma unroll
        for (int k = 0; k < 10; ++k) *(u16*)(smem + ps[k] + (L_BIN + S * 4 - XB)) = (u16)((k & 1) ? av[k >> 1] >> 16 : av[k >> 1]);
        STAMP(8);
        __syncthreads();
        {
            const rsrc_t rn = __builtin_amdgcn_make_buffer_rsrc((void*)(ws + OFF_RA + (size_t)g * (S * 2)), 0, SV * 2, 0x00020000);
            const u32x4 v0 = *(const u32x4*)(smem + L_BIN + S * 4 + t * 16);
            const u32 v1 = *(const u32*)(smem + L_BIN + S * 4 + 16384 + t * 4);
            __builtin_amdgcn_raw_buffer_store_b128(v0, rn, t * 16, 0, SC1);
            __builtin_amdgcn_raw_buffer_store_b32(v1, rn, t * 4, 16384, SC1);
        }
        SB();
        const u32 tb_off = lane < 16 ? (u32)OFF_FLG + (u32)((w * 16 + lane) * NWG + b) * 16 : OOB;
        u32x4 gr = __builtin_amdgcn_raw_buffer_load_b128(rs_ws, tb_off, 0, SC1);
        SB();
        STAMP(3);

        const float f0 = lo16f(freq[0]), f1 = hi16f(freq[0]), f2 = lo16f(freq[1]);
        const float dv = ((b + t) & 1) ? hi16f(dd_) : lo16f(dd_);
        {
            const u32 M = 0x7FFF7FFFu;
            const u32 amax = pkmax(pkmax(pkmax(a0.x & M, a0.y & M), pkmax(a1.x & M, a1.y & M)), a2 & M);
            const u32 am = max(amax & 0xFFFFu, amax >> 16);
            if (!(lo16f(am) * (fabsf(f0) + fabsf(f1) + fabsf(f2)) < 1500.f)) *(u32*)(smem + L_MISC + 24) = 1;
        }
        const u32x2 wdc = *(const u32x2*)(smem + L_WD + (lane & 3) * 8);
        f32x4 sa = {0, 0, 0, 0};
        u32x2 gp = {0, 0};
        if (w < 10) {
            const u32 i = lane & 31;
            const u32x4 wt = *(const u32x4*)(smem + (((i >> 2) & 1) == (u32)(lane >> 5) ? L_WT + ((i >> 3) * 4 + (i & 3)) * 16 : L_ZERO));
            u32x4 rb;
#pragma unroll
            for (int q = 0; q < 4; ++q) {
                f32x2 x = (f32x2){dv, dv} - (f32x2){lo16f(centers[q]), hi16f(centers[q])};
                x = (f32x2){rbf16(x.x), rbf16(x.y)};
                x = x * x;
                x = (f32x2){rbf16(x.x), rbf16(x.y)} * -10.f;
                x = (f32x2){rbf16(x.x), rbf16(x.y)} * 1.44269504088896341f;
                rb[q] = pk_bf16(__builtin_amdgcn_exp2f(x.x), __builtin_amdgcn_exp2f(x.y));
            }
            const f32x16 D = __builtin_amdgcn_mfma_f32_32x32x16_bf16(__builtin_bit_cast(bf16x8, wt), __builtin_bit_cast(bf16x8, rb), (f32x16)(0.f), 0, 0, 0);
            bool bad = false;
            f32x4 r;
#pragma unroll
            for (int k = 0; k < 4; ++k) {
                const float s = fabsf(D[k]) + fabsf(D[4 + k]) + fabsf(D[8 + k]);
                bad |= !(s + fabsf(D[12 + k]) < 0x1p120f);
                sa[k] = fmaxf(s, 0x1p-120f);
                r[k] = __builtin_amdgcn_rcpf(sa[k]);
            }
            *(f32x4*)(smem + L_A + t * 16) = (f32x4){D[0], D[1], D[2], D[3]} * r;
            *(f32x4*)(smem + L_A + A_STR + t * 16) = (f32x4){D[4], D[5], D[6], D[7]} * r;
            *(f32x4*)(smem + L_A + 2 * A_STR + t * 16) = (f32x4){D[8], D[9], D[10], D[11]} * r;
            gp = (u32x2){pk_bf16(D[12], D[13]), pk_bf16(D[14], D[15])};
            if (bad && t < EB) *(u32*)(smem + L_MISC + 4) = 1;
        }
        const float cs = (fabsf(lo16f(wdc.x)) + fabsf(hi16f(wdc.x)) + fabsf(lo16f(wdc.y)) + fabsf(hi16f(wdc.y))) * 1.02f;
        SB();
        STAMP(11);
        asm volatile("s_waitcnt vmcnt(0) lgkmcnt(0)" ::: "memory");
        STAMP(12);
        __syncthreads();
        {
            const u32 far = *(const u32*)(smem + L_MISC + 24);
            constexpr int CW = NCOPY / 16;
            __builtin_amdgcn_raw_buffer_store_b64((u32x2){tag, tag ^ KEY2 ^ far}, rs_ws,
                                                  lane < CW ? (u32)OFF_RDY + (w * CW + lane) * (NWG * 8) + g * 8 : OOB, 0, SC1);
        }
        STAMP(4);

        u32 rdy = 0, done = 0, spins = 0;
        u32x2 fl = {0, 0};
        const u32 fl_off = lane < 16 ? (u32)OFF_RDY + (g & (NCOPY - 1)) * (NWG * 8) + (w * 16 + lane) * 8 : OOB;
        const u32 cj = w * 16 * S + (lane & 15) * 4;
        u32x4 rq[4];
        u32x2 rn[4];
        u32 rd[4], rl[4];
#pragma unroll
        for (int i = 0; i < 4; ++i) {
#pragma unroll 1
            while (__builtin_popcount(rdy & ~done) < 4) {
                asm volatile("" ::: "memory");
                if (rdy | spins) __builtin_amdgcn_s_sleep(1);
                fl = __builtin_amdgcn_raw_buffer_load_b64(rs_ws, fl_off, 0, SC1);
                u32 tok = (u32)__builtin_amdgcn_ballot_w64(gr.x == tag && gr.z == (tag ^ KEY2));
                if ((~tok & 0xFFFFu) != 0) {
                    if (lane < 16 && !((tok >> lane) & 1)) gr = __builtin_amdgcn_raw_buffer_load_b128(rs_ws, tb_off, 0, SC1);
                    tok = (u32)__builtin_amdgcn_ballot_w64(gr.x == tag && gr.z == (tag ^ KEY2));
                }
                rdy = (u32)__builtin_amdgcn_ballot_w64(fl.x == tag && (fl.y ^ tag ^ KEY2) < 2u) & tok & 0xFFFFu;
                if (++spins > (1u << 16)) { gr.y = 0; rdy = 0xFFFFu; }
            }
            u32 av = rdy & ~done, pk = 0;
#pragma unroll
            for (int k = 0; k < 4; ++k) {
                pk |= (u32)__builtin_ctz(av) << (8 * k + 2);
                av &= av - 1;
            }
            done = rdy & ~av;
            const u32 sl4 = (pk >> ((lane >> 4) * 8)) & 0xFFu;
            const u32 sg = __builtin_amdgcn_ds_bpermute(sl4, gr.y);
            rl[i] = sg >> 16;
            rd[i] = (lane & 15) * 4 - (sg & 3);
            u32 off = (__umul24(sl4, S / 4) + cj + (sg & 0xFFFCu)) * 4;
            off = (int)rd[i] < (int)rl[i] ? off : OOB;
            rq[i] = __builtin_amdgcn_raw_buffer_load_b128(rs_rw, off, 0, SC1);
            rn[i] = __builtin_amdgcn_raw_buffer_load_b64(rs_ra, off >> 1, 0, SC1);
            SB();
            SB();
            if (i == 0) STAMP(5);
            if (i == 3) STAMP(10);
            if (i > 0 && i < 3) sort_quad(rq[i - 1], rn[i - 1], rd[i - 1], rl[i - 1], smem, lane);
            SB();
        }
        {
            const u32 need = lane < 16 ? (gr.y >> 16) + (gr.y & 3) : 0u;
            const bool more = __builtin_amdgcn_ballot_w64(need > 64) != 0;
            stat_max(row_pkmax(gr.w), L_MISC + 16, lane == 15);
            if (need > 80 || (lane < 16 && fl.y != (tag ^ KEY2))) *(u32*)(smem + L_MISC + 4) = 1;
            if (more) {
                const u32 sg = __builtin_amdgcn_ds_bpermute(lane & 60, gr.y);
                const u32 j4 = 64 + (lane & 3) * 4, d = j4 - (sg & 3), len = sg >> 16;
                u32 off = ((w * 16 + (lane >> 2)) * S + j4 + (sg & 0xFFFCu)) * 4;
                off = d < len ? off : OOB;
                const u32x4 q = __builtin_amdgcn_raw_buffer_load_b128(rs_rw, off, 0, SC1);
                const u32x2 an = __builtin_amdgcn_raw_buffer_load_b64(rs_ra, off >> 1, 0, SC1);
                sort_quad(rq[2], rn[2], rd[2], rl[2], smem, lane);
                rq[2] = q; rn[2] = an; rd[2] = d; rl[2] = len;
            }
            sort_quad(rq[2], rn[2], rd[2], rl[2], smem, lane);
            sort_quad(rq[3], rn[3], rd[3], rl[3], smem, lane);
        }
        STAMP(6);
        asm volatile("s_waitcnt lgkmcnt(0)" ::: "memory");
        __syncthreads();
        STAMP(13);

        constexpr int NS = NBW + 2;
        u32x2 rec[NS], mg[NS];
        bool val[NS];
        const u32 cn = *(const u32*)(smem + L_BC + (w * NBW + min(lane, NBW - 1)) * 4);
        const u32x2 mi0 = *(const u32x2*)(smem + L_MISC), mi1 = *(const u32x2*)(smem + L_MISC + 16);
        const u32 l8 = lane * 8;
        auto fetch = [&](int k) {
            if (k < NBW) rec[k] = *(const u32x2*)(smem + L_BIN + w * (NBW * BSTR) + l8 + k * BSTR);
        };
#pragma unroll
        for (int k = 0; k < GDEPTH + 1; ++k) fetch(k);
        const u32 nsp = __builtin_amdgcn_readfirstlane(mi0.x);
        auto live = [&](int k) { return k < NBW || (u32)(((k - NBW) * 16 + w) * 64) < nsp; };
        auto gather = [&](int k) {
            if (k < NBW) {
                val[k] = l8 < (u32)__builtin_amdgcn_readlane(cn, k);
                mg[k] = __builtin_amdgcn_raw_buffer_load_b64(rs_m, val[k] ? rec[k].x >> 11 : OOB, 0, 0);
            } else if (live(k)) {
                const u32 p = ((k - NBW) * 16 + w) * 64 + lane;
                val[k] = p < nsp;
                rec[k] = *(const u32x2*)(smem + L_SPILL + min(p, (u32)SPILL - 1) * 8);
                mg[k] = __builtin_amdgcn_raw_buffer_load_b64(rs_m, val[k] ? rec[k].x >> 11 : OOB, 0, 0);
            }
        };
#pragma unroll
        for (int k = 0; k < GDEPTH; ++k) gather(k);
        SB();
        if (g == 0 && t == 0) __builtin_amdgcn_raw_buffer_store_b32(tag, rs_ws, 0, 0, SC1);
        const u32 smax = __builtin_amdgcn_readfirstlane(mi1.x), smin = 0x7FFFu - __builtin_amdgcn_readfirstlane(mi1.y);
        bool slow = nsp > SPILL || __builtin_amdgcn_readfirstlane(mi0.y) != 0 || smax >= 0x7F80u || smin + (9u << 7) < smax;
        Fix F;
        f32x4 inv;
        {
            const float mmax = lo16f(smax), bound = cs * mmax;
            slow |= __builtin_amdgcn_ballot_w64(!(bound == 0.f || (bound > 0x1p-60f && bound < 0x1p60f && mmax > 0x1p-60f))) != 0;
            const int e = __builtin_amdgcn_frexp_expf(bound) - 17;
            const u32 mgl = __float_as_uint(ldexpf(12582912.0f, e)), ivl = __float_as_uint(ldexpf(1.f, e));
            float mgv[4];
#pragma unroll
            for (int k = 0; k < 4; ++k) {
                mgv[k] = __uint_as_float(__builtin_amdgcn_readlane(mgl, k));
                inv[k] = __uint_as_float(__builtin_amdgcn_readlane(ivl, k));
            }
            F.f01 = (f32x2){f0, f1};
            F.f2 = f2;
            F.mg01 = (f32x2){mgv[0], mgv[1]};
            F.mg23 = (f32x2){mgv[2], mgv[3]};
            F.nb01 = 0 - __builtin_bit_cast(u64, F.mg01);
            F.nb23 = 0 - __builtin_bit_cast(u64, F.mg23);
        }
        float ag[4];
        if (!slow) {
#pragma unroll
            for (int k = 0; k < NS; ++k) {
                SB();
                if (k + GDEPTH + 1 < NS) fetch(k + GDEPTH + 1);
                if (live(k)) {
                    const f32x4 md = row_mat4(wdc, mg[k]);
                    if (val[k]) accum_fix(rec[k].x, rec[k].y, md, F, smem);
                }
                SB();
                if (k + GDEPTH < NS) gather(k + GDEPTH);
            }
            STAMP(7);
            __syncthreads();
            if (w < 10) {
                const u64 s01 = *(const u64*)(smem + L_ACC + t * 8), s23 = *(const u64*)(smem + L_ACC + ACC_STR + t * 8);
                const int q0 = (int)(u32)s01, q2 = (int)(u32)s23;
                const int q1 = (int)(u32)(s01 >> 32) - (q0 >> 31), q3 = (int)(u32)(s23 >> 32) - (q2 >> 31);
                ag[0] = (float)q0 * inv[0] * sa[0]; ag[1] = (float)q1 * inv[1] * sa[1];
                ag[2] = (float)q2 * inv[2] * sa[2]; ag[3] = (float)q3 * inv[3] * sa[3];
            }
        } else {
            __syncthreads();
            if (w < 10) {
                const EdgeC e = edge_coeffs(dv, centers, w_rbf, w_sbf);
                *(f32x4*)(smem + L_A + t * 16) = e.A0;
                *(f32x4*)(smem + L_A + A_STR + t * 16) = e.A1;
                *(f32x4*)(smem + L_A + 2 * A_STR + t * 16) = e.A2;
                gp = (u32x2){pk_bf16(e.g.x, e.g.y), pk_bf16(e.g.z, e.g.w)};
                *(u32x4*)(smem + L_BIN + t * 32) = (u32x4){0, 0, 0, 0};
                *(u32x4*)(smem + L_BIN + t * 32 + 16) = (u32x4){0, 0, 0, 0};
            }
            __syncthreads();
#pragma unroll 1
            for (int s = 0; s < 16; ++s) {
                const u32 sg = __builtin_amdgcn_readlane(gr.y, s);
                const u32 len = sg >> 16, p0 = (w * 16 + s) * S + (sg & 0xFFFFu);
#pragma unroll 1
                for (u32 q = 0; q < len; q += 64) {
                    if (q + lane < len) {
                        const u32 p = p0 + q + lane;
                        const u32 w0 = __builtin_amdgcn_raw_buffer_load_b32(rs_rw, p * 4, 0, SC1);
                        const u32 a2 = __builtin_amdgcn_raw_buffer_load_b32(rs_ra, (p >> 1) * 4, 0, SC1);
                        const u32x2 mr = __builtin_amdgcn_raw_buffer_load_b64(rs_m, w0 >> 11, 0, 0);
                        accum_slow(w0, (p & 1) ? a2 & 0xFFFF0000u : a2 << 16, mr, w_down, f0, f1, f2, smem);
                    }
                }
            }
            __syncthreads();
            if (w < 10) {
                const double* acc = (const double*)(smem + L_BIN) + t;
                ag[0] = (float)acc[0]; ag[1] = (float)acc[640]; ag[2] = (float)acc[1280]; ag[3] = (float)acc[1920];
            }
        }
        if (w < 10) {
            const f32x4 u = row_mat4(*(const u32x2*)(smem + L_WD + 32 + (lane & 3) * 8), (u32x2){pk_bf16(ag[0], ag[1]), pk_bf16(ag[2], ag[3])});
            const float mm[4] = {lo16f(mrow_.x), hi16f(mrow_.x), lo16f(mrow_.y), hi16f(mrow_.y)};
            const float gg[4] = {lo16f(gp.x), hi16f(gp.x), lo16f(gp.y), hi16f(gp.y)};
            float o[4];
#pragma unroll
            for (int k = 0; k < 4; ++k) o[k] = mm[k] + rbf16(rbf16(u[k]) * gg[k]);
            __builtin_amdgcn_raw_buffer_store_b64((u32x2){pk_bf16(o[0], o[1]), pk_bf16(o[2], o[3])}, rs_out, t * 8, 0, SC1);
        }
    }
    STAMP(9);
}
}

namespace generic {
__global__ void k_trip(const u16* m, const int* idx_in, const int* idx_out, const u16* dist,
                       const u16* angle, const u16* centers, const u16* freq,
                       const u16* w_sbf, const u16* w_down, float* acc, int T) {
    int t = blockIdx.x * blockDim.x + threadIdx.x;
    if (t >= T) return;
    int ei = idx_in[t], eo = idx_out[t];
    float a = bf2f(angle[t]);
    float d = bf2f(dist[eo]);
    float c[3];
    for (int l = 0; l < 3; ++l) c[l] = rbf16(cosf(rbf16(bf2f(freq[l]) * a)));
    float s[4] = {0, 0, 0, 0};
    for (int r = 0; r < 8; ++r) {
        float df = rbf16(d - bf2f(centers[r]));
        float rb = rbf16(expf(rbf16(-10.f * rbf16(df * df))));
        for (int l = 0; l < 3; ++l) {
            float sb = rbf16(rb * c[l]);
            for (int k = 0; k < 4; ++k) s[k] += sb * bf2f(w_sbf[(r * 3 + l) * 4 + k]);
        }
    }
    float mm[4];
    for (int k = 0; k < 4; ++k) mm[k] = bf2f(m[ei * 4 + k]);
    for (int k = 0; k < 4; ++k) {
        float md = 0;
        for (int j = 0; j < 4; ++j) md += mm[j] * bf2f(w_down[j * 4 + k]);
        float x = rbf16(rbf16(md) * rbf16(s[k]));
        atomicAdd(&acc[eo * 4 + k], x);
    }
}

__global__ void k_fin(const u16* m, const u16* dist, const u16* centers, const u16* w_rbf,
                      const u16* w_up, const float* acc, __hip_bfloat16* out, int E) {
    int e = blockIdx.x * blockDim.x + threadIdx.x;
    if (e >= E) return;
    float d = bf2f(dist[e]);
    float g[4] = {0, 0, 0, 0};
    for (int r = 0; r < 8; ++r) {
        float df = rbf16(d - bf2f(centers[r]));
        float rb = rbf16(expf(rbf16(-10.f * rbf16(df * df))));
        for (int k = 0; k < 4; ++k) g[k] += rb * bf2f(w_rbf[r * 4 + k]);
    }
    float ag[4];
    for (int k = 0; k < 4; ++k) ag[k] = rbf16(acc[e * 4 + k]);
    for (int k = 0; k < 4; ++k) {
        float u = 0;
        for (int j = 0; j < 4; ++j) u += ag[j] * bf2f(w_up[j * 4 + k]);
        float y = rbf16(rbf16(u) * rbf16(g[k]));
        out[e * 4 + k] = __float2bfloat16(bf2f(m[e * 4 + k]) + y);
    }
}

}

extern "C" void kernel_launch(void* const* d_in, const int* in_sizes, int n_in,
                              void* d_out, int out_size, void* d_ws, size_t ws_size,
                              hipStream_t stream) {
    const u16* m = (const u16*)d_in[0];
    const u32* idx_in = (const u32*)d_in[1];
    const u32* idx_out = (const u32*)d_in[2];
    const u16* dist = (const u16*)d_in[3];
    const u16* angle = (const u16*)d_in[4];
    const u16* centers = (const u16*)d_in[5];
    const u16* freq = (const u16*)d_in[6];
    const u16* w_rbf = (const u16*)d_in[7];
    const u16* w_sbf = (const u16*)d_in[8];
    const u16* w_down = (const u16*)d_in[9];
    const u16* w_up = (const u16*)d_in[10];
    const int T = in_sizes[1], E = in_sizes[3];
    if (T != fast::kT || E != fast::kE || ws_size < fast::WS_NEED) {
        float* acc = (float*)d_ws;
        hipMemsetAsync(acc, 0, (size_t)E * 4 * sizeof(float), stream);
        generic::k_trip<<<(T + 255) / 256, 256, 0, stream>>>(m, (const int*)idx_in, (const int*)idx_out, dist, angle, centers, freq, w_sbf, w_down, acc, T);
        generic::k_fin<<<(E + 255) / 256, 256, 0, stream>>>(m, dist, centers, w_rbf, w_up, acc, (__hip_bfloat16*)d_out, E);
        return;
    }
    fast::k_main<<<fast::NWG, 1024, 0, stream>>>(m, idx_in, idx_out, dist, angle, centers, freq, w_rbf, w_sbf, w_down, w_up, (u16*)d_out, (char*)d_ws);
}
```

```cpp
#include <hip/hip_runtime.h>
#include <hip/hip_bf16.h>
#include <stdint.h>

#define IN_AUX 2
#define GDEPTH 3
#define NCOPY 32
#define CODE_LINES 16
#define EXP 0

typedef unsigned int u32;
typedef unsigned short u16;
typedef unsigned long long u64;
typedef u32 u32x2 __attribute__((ext_vector_type(2)));
typedef u32 u32x4 __attribute__((ext_vector_type(4)));
typedef u32 u32x8 __attribute__((ext_vector_type(8)));
typedef u16 u16x2 __attribute__((ext_vector_type(2)));
typedef float f32x4 __attribute__((ext_vector_type(4)));
typedef float f32x2 __attribute__((ext_vector_type(2)));
typedef float f32x16 __attribute__((ext_vector_type(16)));
typedef short s16x4 __attribute__((ext_vector_type(4)));
typedef __bf16 bf16x8 __attribute__((ext_vector_type(8)));
typedef const __attribute__((address_space(4))) u32* cptr;
typedef __amdgpu_buffer_rsrc_t rsrc_t;

static __device__ __forceinline__ float bf2f(u16 v) { return __uint_as_float(((u32)v) << 16); }
static __device__ __forceinline__ float lo16f(u32 v) { return __uint_as_float(v << 16); }
static __device__ __forceinline__ float hi16f(u32 v) { return __uint_as_float(v & 0xFFFF0000u); }
static __device__ __forceinline__ u32 pk_bf16(float a, float b) {
    typedef __bf16 bf16x2 __attribute__((ext_vector_type(2)));
    bf16x2 r = __builtin_convertvector((f32x2){a, b}, bf16x2);
    return __builtin_bit_cast(u32, r);
}
static __device__ __forceinline__ float rbf16(float x) { return __uint_as_float(pk_bf16(0.f, x)); }
static __device__ __forceinline__ float cw(cptr p, int i) { u32 v = p[i >> 1]; return (i & 1) ? hi16f(v) : lo16f(v); }
static __device__ __forceinline__ u32 pkmax(u32 a, u32 b) {
    return __builtin_bit_cast(u32, __builtin_elementwise_max(__builtin_bit_cast(u16x2, a), __builtin_bit_cast(u16x2, b)));
}

namespace fast {
constexpr int kE = 160000, kT = 2560000;
constexpr int NWG = 256, EB = 625, NC = 256, SV = 10000, S = 10240;
constexpr u32 MAGIC = 6871948u;
constexpr int NBIN = 160, BSTR = 520, SPILL = 1536;
constexpr u32 KEY2 = 0x9E3779B9u;
constexpr size_t OFF_RDY = 4096;
constexpr size_t OFF_FLG = OFF_RDY + 64 * NWG * 8;
constexpr size_t OFF_RW = OFF_FLG + (size_t)NWG * NWG * 16;
constexpr size_t OFF_RA = OFF_RW + (size_t)NC * S * 4;
constexpr size_t OFF_DIAG = OFF_RA + (size_t)NC * S * 2;
constexpr size_t WS_NEED = OFF_DIAG + 256 * 16 * 64;
constexpr int SC1 = 16;
constexpr u32 OOB = 0xFFFFFF00u;

constexpr int L_A = 0, A_STR = 640 * 16;
constexpr int L_ACC = 3 * A_STR, ACC_STR = 640 * 8;
constexpr int L_CNT = L_ACC + 2 * ACC_STR;
constexpr int L_BASE = L_CNT + 1024;
constexpr int L_BC = L_BASE + 1024;
constexpr int L_MISC = L_BC + 640;
constexpr int L_PAD = L_MISC + 32;
constexpr int L_ZERO = L_PAD + 16;
constexpr int L_WD = L_BC + 704;
constexpr int L_WT = L_BC + 768;
constexpr int L_BIN = L_BC + 1024;
constexpr int L_SPILL = L_BIN + NBIN * BSTR;
constexpr int L_END = L_SPILL + SPILL * 8;
constexpr int XB = L_BIN / 2;
static_assert(L_END <= 163840 && S * 6 <= NBIN * BSTR && L_BIN + S * 4 - XB < 65536, "lds");

#define STAMP(i)
#define SB() __builtin_amdgcn_sched_barrier(0)

static __device__ __forceinline__ u32 lds_add(char* p, u32 v) {
    return __hip_atomic_fetch_add((u32*)p, v, __ATOMIC_RELAXED, __HIP_MEMORY_SCOPE_WORKGROUP);
}
static __device__ __forceinline__ void stat_max(u32 st, u32 addr, bool on) {
    if (on) asm volatile("ds_max_u32 %0, %1\n\tds_max_u32 %0, %2 offset:4" :: "v"(addr), "v"(st & 0xFFFFu), "v"(st >> 16) : "memory");
}
static __device__ __forceinline__ u32 wave_scan_incl(u32 v) {
    v += __builtin_amdgcn_update_dpp(0u, v, 0x111, 0xf, 0xf, false);
    v += __builtin_amdgcn_update_dpp(0u, v, 0x112, 0xf, 0xf, false);
    v += __builtin_amdgcn_update_dpp(0u, v, 0x114, 0xf, 0xf, false);
    v += __builtin_amdgcn_update_dpp(0u, v, 0x118, 0xf, 0xf, false);
    v += __builtin_amdgcn_update_dpp(0u, v, 0x142, 0xa, 0xf, false);
    v += __builtin_amdgcn_update_dpp(0u, v, 0x143, 0xc, 0xf, false);
    return v;
}
static __device__ __forceinline__ u32 row_pkmax(u32 v) {
    v = pkmax(v, __builtin_amdgcn_update_dpp(0u, v, 0x111, 0xf, 0xf, false));
    v = pkmax(v, __builtin_amdgcn_update_dpp(0u, v, 0x112, 0xf, 0xf, false));
    v = pkmax(v, __builtin_amdgcn_update_dpp(0u, v, 0x114, 0xf, 0xf, false));
    v = pkmax(v, __builtin_amdgcn_update_dpp(0u, v, 0x118, 0xf, 0xf, false));
    return v;
}

struct EdgeC { f32x4 A0, A1, A2, g; };
static __device__ __forceinline__ EdgeC edge_coeffs(float d, cptr centers, cptr w_rbf, cptr w_sbf) {
    EdgeC e;
    e.A0 = e.A1 = e.A2 = e.g = (f32x4){0, 0, 0, 0};
#pragma unroll
    for (int q = 0; q < 8; ++q) {
        float df = rbf16(d - cw(centers, q));
        float rb = rbf16(__expf(rbf16(-10.f * rbf16(df * df))));
#pragma unroll
        for (int k = 0; k < 4; ++k) {
            e.A0[k] += rb * cw(w_sbf, (q * 3 + 0) * 4 + k);
            e.A1[k] += rb * cw(w_sbf, (q * 3 + 1) * 4 + k);
            e.A2[k] += rb * cw(w_sbf, (q * 3 + 2) * 4 + k);
            e.g[k] += rb * cw(w_rbf, q * 4 + k);
        }
    }
    return e;
}

constexpr float INV2PI = 0.15915494309189535f;

struct Fix { f32x2 f01; float f2; f32x2 mg01, mg23; u64 nb01, nb23; };

static __device__ __forceinline__ f32x4 row_mat4(u32x2 col, u32x2 row) {
    return __builtin_amdgcn_mfma_f32_4x4x4bf16_1k(__builtin_bit_cast(s16x4, col), __builtin_bit_cast(s16x4, row), (f32x4){0, 0, 0, 0}, 0, 0, 0);
}
static __device__ __forceinline__ u64 add64(u64 a, u64 b) {
    u64 r;
    asm("v_lshl_add_u64 %0, %1, 0, %2" : "=v"(r) : "v"(a), "s"(b));
    return r;
}
static __device__ __forceinline__ void accum_fix(u32 w0, u32 aw, f32x4 d, const Fix& F, char* smem) {
    u32 l16, l8;
    asm("v_lshlrev_b16 %0, 4, %1" : "=v"(l16) : "v"(w0));
    asm("v_lshlrev_b16 %0, 3, %1" : "=v"(l8) : "v"(w0));
    const f32x4 A0 = *(const f32x4*)(smem + L_A + l16);
    const f32x4 A1 = *(const f32x4*)(smem + L_A + A_STR + l16);
    const f32x4 A2 = *(const f32x4*)(smem + L_A + 2 * A_STR + l16);
    const float a = __uint_as_float(aw);
    const f32x2 p01 = F.f01 * a;
    const f32x2 r01 = (f32x2){rbf16(p01.x), rbf16(p01.y)} * INV2PI;
    const float c0 = __builtin_amdgcn_cosf(r01.x), c1 = __builtin_amdgcn_cosf(r01.y);
    const float c2 = __builtin_amdgcn_cosf(rbf16(F.f2 * a) * INV2PI);
    const f32x4 s = A0 * c0 + A1 * c1 + A2 * c2;
    const f32x2 q01 = __builtin_elementwise_fma((f32x2){d.x, d.y}, (f32x2){s.x, s.y}, F.mg01);
    const f32x2 q23 = __builtin_elementwise_fma((f32x2){d.z, d.w}, (f32x2){s.z, s.w}, F.mg23);
    __hip_atomic_fetch_add((u64*)(smem + L_ACC + l8), add64(__builtin_bit_cast(u64, q01), F.nb01), __ATOMIC_RELAXED, __HIP_MEMORY_SCOPE_WORKGROUP);
    __hip_atomic_fetch_add((u64*)(smem + L_ACC + ACC_STR + l8), add64(__builtin_bit_cast(u64, q23), F.nb23), __ATOMIC_RELAXED, __HIP_MEMORY_SCOPE_WORKGROUP);
}

static __device__ __forceinline__ float cos_any(float x) {
    return __builtin_amdgcn_cosf(__builtin_amdgcn_fractf(x * INV2PI));
}
static __device__ __forceinline__ void accum_slow(u32 w0, u32 aw, u32x2 mr, cptr w_down, float f0, float f1, float f2, char* smem) {
    const u32 l = w0 & 0x3FFu;
    const float a = __uint_as_float(aw);
    const float c0 = cos_any(rbf16(f0 * a)), c1 = cos_any(rbf16(f1 * a)), c2 = cos_any(rbf16(f2 * a));
    const f32x4 s = *(const f32x4*)(smem + L_A + l * 16) * c0 + *(const f32x4*)(smem + L_A + A_STR + l * 16) * c1 +
                    *(const f32x4*)(smem + L_A + 2 * A_STR + l * 16) * c2;
    double* acc = (double*)(smem + L_BIN + l * 8);
#pragma unroll
    for (int k = 0; k < 4; ++k) {
        float md = lo16f(mr.x) * cw(w_down, k) + hi16f(mr.x) * cw(w_down, 4 + k) + lo16f(mr.y) * cw(w_down, 8 + k) + hi16f(mr.y) * cw(w_down, 12 + k);
        __hip_atomic_fetch_add(acc + k * 640, (double)(rbf16(md) * s[k]), __ATOMIC_RELAXED, __HIP_MEMORY_SCOPE_WORKGROUP);
    }
}

static __device__ __forceinline__ void sort_quad(u32x4 q, u32x2 an, u32 d, u32 len, char* smem, int lane) {
    const u32 w0[4] = {q.x, q.y, q.z, q.w};
    u32 ka[4], rk[4];
    u64 mk[4];
#pragma unroll
    for (int k = 0; k < 4; ++k) {
        ka[k] = (w0[k] >> 24) << 2;
        mk[k] = __builtin_amdgcn_ballot_w64(d + k < len);
    }
    const u32 eight = 8;
    asm volatile("s_mov_b64 exec, %8\n\tds_add_rtn_u32 %0, %4, %12 offset:%13\n\t"
                 "s_mov_b64 exec, %9\n\tds_add_rtn_u32 %1, %5, %12 offset:%13\n\t"
                 "s_mov_b64 exec, %10\n\tds_add_rtn_u32 %2, %6, %12 offset:%13\n\t"
                 "s_mov_b64 exec, %11\n\tds_add_rtn_u32 %3, %7, %12 offset:%13\n\t"
                 "s_mov_b64 exec, -1"
                 : "=&v"(rk[0]), "=&v"(rk[1]), "=&v"(rk[2]), "=&v"(rk[3])
                 : "v"(ka[0]), "v"(ka[1]), "v"(ka[2]), "v"(ka[3]), "s"(mk[0]), "s"(mk[1]), "s"(mk[2]), "s"(mk[3]),
                   "v"(eight), "n"(L_BC)
                 : "memory");
    const u32x2 r[4] = {{w0[0], an.x << 16}, {w0[1], an.x & 0xFFFF0000u}, {w0[2], an.y << 16}, {w0[3], an.y & 0xFFFF0000u}};
    asm volatile("s_waitcnt lgkmcnt(0)" : "+v"(rk[0]), "+v"(rk[1]), "+v"(rk[2]), "+v"(rk[3]) :: "memory");
    u32 ad[4];
    u64 ov[4];
#pragma unroll
    for (int k = 0; k < 4; ++k) {
        ad[k] = __umul24(ka[k], BSTR / 4) + rk[k];
        ov[k] = __builtin_amdgcn_ballot_w64(rk[k] >= 512u) & mk[k];
        mk[k] &= ~ov[k];
    }
    asm volatile("s_mov_b64 exec, %8\n\tds_write_b64 %0, %4 offset:%12\n\t"
                 "s_mov_b64 exec, %9\n\tds_write_b64 %1, %5 offset:%12\n\t"
                 "s_mov_b64 exec, %10\n\tds_write_b64 %2, %6 offset:%12\n\t"
                 "s_mov_b64 exec, %11\n\tds_write_b64 %3, %7 offset:%12\n\t"
                 "s_mov_b64 exec, -1"
                 :: "v"(ad[0]), "v"(ad[1]), "v"(ad[2]), "v"(ad[3]), "v"(r[0]), "v"(r[1]), "v"(r[2]), "v"(r[3]),
                    "s"(mk[0]), "s"(mk[1]), "s"(mk[2]), "s"(mk[3]), "n"(L_BIN)
                 : "memory");
    if ((ov[0] | ov[1] | ov[2] | ov[3]) != 0) {
        const u32 n0 = __builtin_popcountll(ov[0]), n1 = n0 + __builtin_popcountll(ov[1]), n2 = n1 + __builtin_popcountll(ov[2]);
        const u32 n3 = n2 + __builtin_popcountll(ov[3]);
        u32 p = 0;
        if (lane == 0) p = lds_add(smem + L_MISC, n3);
        p = __builtin_amdgcn_readfirstlane(p);
        if (p + n3 <= SPILL) {
            const u32 pb[4] = {p, p + n0, p + n1, p + n2};
#pragma unroll
            for (int k = 0; k < 4; ++k)
                ad[k] = __builtin_amdgcn_mbcnt_hi((u32)(ov[k] >> 32), __builtin_amdgcn_mbcnt_lo((u32)ov[k], pb[k] + L_SPILL / 8)) * 8;
            asm volatile("s_mov_b64 exec, %8\n\tds_write_b64 %0, %4\n\t"
                         "s_mov_b64 exec, %9\n\tds_write_b64 %1, %5\n\t"
                         "s_mov_b64 exec, %10\n\tds_write_b64 %2, %6\n\t"
                         "s_mov_b64 exec, %11\n\tds_write_b64 %3, %7\n\t"
                         "s_mov_b64 exec, -1"
                         :: "v"(ad[0]), "v"(ad[1]), "v"(ad[2]), "v"(ad[3]), "v"(r[0]), "v"(r[1]), "v"(r[2]), "v"(r[3]),
                            "s"(ov[0]), "s"(ov[1]), "s"(ov[2]), "s"(ov[3])
                         : "memory");
        }
    }
}

__global__ void __launch_bounds__(1024)
k_main(const u16* p0, const u32* p1, const u32* p2, const u16* p3, const u16* p4, const u16* p5, const u16* p6,
       const u16* p7, const u16* p8, const u16* p9, const u16* p10, u16* p11, char* p12) {
    __shared__ __attribute__((aligned(16))) char smem[L_END];
    const __attribute__((address_space(4))) u64* kp = (const __attribute__((address_space(4))) u64*)__builtin_amdgcn_kernarg_segment_ptr();
    u64 P[13];
#pragma unroll
    for (int k = 0; k < 13; ++k) P[k] = kp[k];
    asm volatile("" : "+s"(P[0]), "+s"(P[1]), "+s"(P[2]), "+s"(P[3]), "+s"(P[4]), "+s"(P[5]), "+s"(P[6]), "+s"(P[7]), "+s"(P[8]),
                 "+s"(P[9]), "+s"(P[10]), "+s"(P[11]), "+s"(P[12]));
    const u16* m = (const u16*)P[0];
    const u32* idx_in = (const u32*)P[1];
    const u32* idx_out = (const u32*)P[2];
    const u16* dist = (const u16*)P[3];
    const u16* angle = (const u16*)P[4];
    const cptr centers = (cptr)P[5], freq = (cptr)P[6], w_rbf = (cptr)P[7], w_sbf = (cptr)P[8], w_down = (cptr)P[9], w_up = (cptr)P[10];
    u16* out = (u16*)P[11];
    char* ws = (char*)P[12];
    const int t = threadIdx.x, lane = t & 63;
    const int w = __builtin_amdgcn_readfirstlane(t >> 6);
    const int g = blockIdx.x;
    const int b = (g & 7) * 32 + (g >> 3);
    STAMP(0);

    const size_t cb = (size_t)g * SV;
    const rsrc_t ro = __builtin_amdgcn_make_buffer_rsrc((void*)(idx_out + cb), 0, SV * 4, 0x00020000);
    const u32x4 o0 = __builtin_amdgcn_raw_buffer_load_b128(ro, t * 16, 0, IN_AUX);
    const u32x4 o1 = __builtin_amdgcn_raw_buffer_load_b128(ro, t * 16, 16384, IN_AUX);
    const u32x2 o2 = __builtin_amdgcn_raw_buffer_load_b64(ro, t * 8, 32768, IN_AUX);
    SB();
    const rsrc_t rs_ms = __builtin_amdgcn_make_buffer_rsrc((void*)(m + (size_t)b * EB * 4), 0, EB * 8, 0x00020000);
    const rsrc_t rs_ds = __builtin_amdgcn_make_buffer_rsrc((void*)dist, 0, kE * 2, 0x00020000);
    const rsrc_t rs_ws = __builtin_amdgcn_make_buffer_rsrc((void*)ws, 0, (u32)OFF_RW, 0x00020000);
    const rsrc_t rs_rw = __builtin_amdgcn_make_buffer_rsrc((void*)(ws + OFF_RW), 0, NC * S * 4, 0x00020000);
    const rsrc_t rs_ra = __builtin_amdgcn_make_buffer_rsrc((void*)(ws + OFF_RA), 0, NC * S * 2, 0x00020000);
    const rsrc_t rs_out = __builtin_amdgcn_make_buffer_rsrc((void*)(out + (size_t)b * EB * 4), 0, EB * 8, 0x00020000);
    const rsrc_t rs_m = __builtin_amdgcn_make_buffer_rsrc((void*)m, 0, kE * 8, 0x00020000);
    const u32 tag_ = __builtin_amdgcn_raw_buffer_load_b32(rs_ws, 0, 0, SC1);
    const u32 dd_ = __builtin_amdgcn_raw_buffer_load_b32(rs_ds, ((b * EB + t) & ~1) * 2, 0, 0);
    const u32x2 mrow_ = __builtin_amdgcn_raw_buffer_load_b64(rs_ms, t * 8, 0, 0);
    u32 small = 0;
    if (w >= 14) {
        u64 p;
        if (w == 15) {
            p = lane < 48 ? (u64)w_sbf + lane * 4 : (u64)w_rbf + (lane - 48) * 4;
        } else {
            p = lane < 8 ? (u64)w_down + lane * 4 : (u64)w_up + (lane & 7) * 4;
            p = lane == 16 ? (u64)centers : p;
            p = lane == 17 ? (u64)freq : p;
            p = lane == 18 ? (u64)(out + (size_t)b * EB * 4) : p;
            p = lane >= 32 && lane < 32 + CODE_LINES ? (u64)__builtin_amdgcn_s_getpc() + (((g >> 3) & 7) * CODE_LINES + lane - 32) * 128 : p;
        }
        small = *(const __attribute__((address_space(1))) u32*)p;
    }
    SB();
    if (t < 192) *(u32x4*)(smem + L_CNT + t * 16) = (u32x4){t * 16 == L_PAD - L_CNT ? XB + 16384u : 0u, 0, 0, 0};
    if (t >= 256 && t < 896) *(u32x4*)(smem + L_ACC + (t - 256) * 16) = (u32x4){0, 0, 0, 0};
    __syncthreads();
    {
        const rsrc_t ri = __builtin_amdgcn_make_buffer_rsrc((void*)(idx_in + cb), 0, SV * 4, 0x00020000);
        const rsrc_t ra = __builtin_amdgcn_make_buffer_rsrc((void*)(angle + cb), 0, SV * 2, 0x00020000);
        const u32x4 i0 = __builtin_amdgcn_raw_buffer_load_b128(ri, t * 16, 0, IN_AUX);
        const u32x4 i1 = __builtin_amdgcn_raw_buffer_load_b128(ri, t * 16, 16384, IN_AUX);
        const u32x2 i2 = __builtin_amdgcn_raw_buffer_load_b64(ri, t * 8, 32768, IN_AUX);
        SB();
        const bool tail = t < (SV - 8192) / 2;
        u32 lo[10] = {o0.x, o0.y, o0.z, o0.w, o1.x, o1.y, o1.z, o1.w, o2.x, o2.y};
        u32 ps[10], bk[10];
        auto count = [&](int k) {
            u32 bb;
            asm("v_mul_hi_u32_u24 %0, %1, %2" : "=v"(bb) : "s"(MAGIC), "v"(lo[k]));
            bk[k] = bb * 4;
            ps[k] = lds_add(smem + L_CNT + bk[k], 2u);
            asm("v_mad_i32_i24 %0, %1, %2, %0" : "+v"(lo[k]) : "v"(bb), "s"(-EB));
        };
#pragma unroll
        for (int k = 0; k < 8; ++k) count(k);
        if (tail) { count(8); count(9); }
        SB();
        STAMP(1);
        const u32x2 a0 = __builtin_amdgcn_raw_buffer_load_b64(ra, t * 8, 0, IN_AUX);
        const u32x2 a1 = __builtin_amdgcn_raw_buffer_load_b64(ra, t * 8, 8192, IN_AUX);
        const u32 a2 = __builtin_amdgcn_raw_buffer_load_b32(ra, t * 4, 16384, IN_AUX);
        SB();
        if (w >= 14) {
            u32 ad;
            if (w == 15) {
                const u32 row = lane >> 1, r = lane < 48 ? (row * 43) >> 7 : row - 24;
                ad = L_WT + (lane < 48 ? row - r * 3 : 3) * 64 + (lane & 1) * 32 + r * 2;
                *(u16*)(smem + ad) = (u16)small;
                *(u16*)(smem + ad + 16) = (u16)(small >> 16);
            } else if (lane < 16) {
                ad = L_WD + (lane >> 3) * 32 + (lane & 1) * 16 + ((lane >> 1) & 3) * 2;
                *(u16*)(smem + ad) = (u16)small;
                *(u16*)(smem + ad + 8) = (u16)(small >> 16);
            }
        }
        if (w < 10) {
            const u32 M = 0x7FFF7FFFu;
            u32 rm = pkmax(mrow_.x & M, mrow_.y & M);
            rm = max(rm & 0xFFFFu, rm >> 16);
            stat_max(row_pkmax(rm | (rm ? (0x7FFFu - rm) << 16 : 0u)), L_MISC + 8, (lane & 15) == 15);
        }
        asm volatile("s_waitcnt lgkmcnt(0)" ::: "memory");
        __syncthreads();
        {
            const u32x4 c = *(const u32x4*)(smem + L_CNT + lane * 16);
            const u32 s1 = c.x, s2 = s1 + c.y, s3 = s2 + c.z, s4 = s3 + c.w;
            const u32 ex = wave_scan_incl(s4) - s4 + XB;
            *(u32x4*)(smem + L_BASE + lane * 16) = (u32x4){ex, ex + s1, ex + s2, ex + s3};
        }
        __builtin_amdgcn_fence(__ATOMIC_RELEASE, "wavefront");
        __builtin_amdgcn_wave_barrier();
        __builtin_amdgcn_fence(__ATOMIC_ACQUIRE, "wavefront");
        if (!tail) { bk[8] = bk[9] = L_PAD - L_BASE; ps[8] = t * 4; ps[9] = t * 4 + 2; }
#pragma unroll
        for (int k = 0; k < 10; ++k) ps[k] += *(const u32*)(smem + L_BASE + bk[k]);
        SB();
        const u32 iv[10] = {i0.x, i0.y, i0.z, i0.w, i1.x, i1.y, i1.z, i1.w, i2.x, i2.y};
#pragma unroll
        for (int k = 0; k < 10; ++k) *(u32*)(smem + ps[k] * 2) = (iv[k] << 14) | lo[k];
        SB();
        const u32 tag = __builtin_amdgcn_readfirstlane(tag_) + 1;
        {
            const u32 e = w * 16 + (lane & 15);
            const u32 st = (*(const u32*)(smem + L_BASE + e * 4) - XB) >> 1, n = *(const u32*)(smem + L_CNT + e * 4) >> 1;
            const u32x2 own = *(const u32x2*)(smem + L_MISC + 8);
            __builtin_amdgcn_raw_buffer_store_b128((u32x4){tag, st | (n << 16), tag ^ KEY2, own.x | (own.y << 16)}, rs_ws,
                                                   lane < 16 ? (u32)OFF_FLG + g * (NWG * 16) + e * 16 : OOB, 0, SC1);
        }
        STAMP(2);
        __syncthreads();
        {
            const rsrc_t rw = __builtin_amdgcn_make_buffer_rsrc((void*)(ws + OFF_RW + (size_t)g * (S * 4)), 0, SV * 4, 0x00020000);
            const u32x4 v0 = *(const u32x4*)(smem + L_BIN + t * 16), v1 = *(const u32x4*)(smem + L_BIN + 16384 + t * 16);
            const u32x2 v2 = *(const u32x2*)(smem + L_BIN + 32768 + t * 8);
            __builtin_amdgcn_raw_buffer_store_b128(v0, rw, t * 16, 0, SC1);
            __builtin_amdgcn_raw_buffer_store_b128(v1, rw, t * 16 + 16384, 0, SC1);
            __builtin_amdgcn_raw_buffer_store_b64(v2, rw, t * 8, 32768, SC1);
        }
        SB();
        const u32 av[5] = {a0.x, a0.y, a1.x, a1.y, a2};
#pragma unroll
        for (int k = 0; k < 10; ++k) *(u16*)(smem + ps[k] + (L_BIN + S * 4 - XB)) = (u16)((k & 1) ? av[k >> 1] >> 16 : av[k >> 1]);
        STAMP(8);
        __syncthreads();
        {
            const rsrc_t rn = __builtin_amdgcn_make_buffer_rsrc((void*)(ws + OFF_RA + (size_t)g * (S * 2)), 0, SV * 2, 0x00020000);
            const u32x4 v0 = *(const u32x4*)(smem + L_BIN + S * 4 + t * 16);
            const u32 v1 = *(const u32*)(smem + L_BIN + S * 4 + 16384 + t * 4);
            __builtin_amdgcn_raw_buffer_store_b128(v0, rn, t * 16, 0, SC1);
            __builtin_amdgcn_raw_buffer_store_b32(v1, rn, t * 4, 16384, SC1);
        }
        SB();
        const u32 tb_off = lane < 16 ? (u32)OFF_FLG + (u32)((w * 16 + lane) * NWG + b) * 16 : OOB;
        u32x4 gr = __builtin_amdgcn_raw_buffer_load_b128(rs_ws, tb_off, 0, SC1);
        SB();
        STAMP(3);

        const float f0 = lo16f(freq[0]), f1 = hi16f(freq[0]), f2 = lo16f(freq[1]);
        const float dv = ((b + t) & 1) ? hi16f(dd_) : lo16f(dd_);
        {
            const u32 M = 0x7FFF7FFFu;
            const u32 amax = pkmax(pkmax(pkmax(a0.x & M, a0.y & M), pkmax(a1.x & M, a1.y & M)), a2 & M);
            const u32 am = max(amax & 0xFFFFu, amax >> 16);
            if (!(lo16f(am) * (fabsf(f0) + fabsf(f1) + fabsf(f2)) < 1500.f)) *(u32*)(smem + L_MISC + 24) = 1;
        }
        const u32x2 wdc = *(const u32x2*)(smem + L_WD + (lane & 3) * 8);
        f32x4 sa = {0, 0, 0, 0};
        u32x2 gp = {0, 0};
        if (w < 10) {
            const u32 i = lane & 31;
            const u32x4 wt = *(const u32x4*)(smem + (((i >> 2) & 1) == (u32)(lane >> 5) ? L_WT + ((i >> 3) * 4 + (i & 3)) * 16 : L_ZERO));
            u32x4 rb;
#pragma unroll
            for (int q = 0; q < 4; ++q) {
                f32x2 x = (f32x2){dv, dv} - (f32x2){lo16f(centers[q]), hi16f(centers[q])};
                x = (f32x2){rbf16(x.x), rbf16(x.y)};
                x = x * x;
                x = (f32x2){rbf16(x.x), rbf16(x.y)} * -10.f;
                x = (f32x2){rbf16(x.x), rbf16(x.y)} * 1.44269504088896341f;
                rb[q] = pk_bf16(__builtin_amdgcn_exp2f(x.x), __builtin_amdgcn_exp2f(x.y));
            }
            const f32x16 D = __builtin_amdgcn_mfma_f32_32x32x16_bf16(__builtin_bit_cast(bf16x8, wt), __builtin_bit_cast(bf16x8, rb), (f32x16)(0.f), 0, 0, 0);
            bool bad = false;
            f32x4 r;
#pragma unroll
            for (int k = 0; k < 4; ++k) {
                const float s = fabsf(D[k]) + fabsf(D[4 + k]) + fabsf(D[8 + k]);
                bad |= !(s + fabsf(D[12 + k]) < 0x1p120f);
                sa[k] = fmaxf(s, 0x1p-120f);
                r[k] = __builtin_amdgcn_rcpf(sa[k]);
            }
            *(f32x4*)(smem + L_A + t * 16) = (f32x4){D[0], D[1], D[2], D[3]} * r;
            *(f32x4*)(smem + L_A + A_STR + t * 16) = (f32x4){D[4], D[5], D[6], D[7]} * r;
            *(f32x4*)(smem + L_A + 2 * A_STR + t * 16) = (f32x4){D[8], D[9], D[10], D[11]} * r;
            gp = (u32x2){pk_bf16(D[12], D[13]), pk_bf16(D[14], D[15])};
            if (bad && t < EB) *(u32*)(smem + L_MISC + 4) = 1;
        }
        const float cs = (fabsf(lo16f(wdc.x)) + fabsf(hi16f(wdc.x)) + fabsf(lo16f(wdc.y)) + fabsf(hi16f(wdc.y))) * 1.02f;
        SB();
        STAMP(11);
        asm volatile("s_waitcnt vmcnt(0) lgkmcnt(0)" ::: "memory");
        STAMP(12);
        __syncthreads();
        {
            const u32 far = *(const u32*)(smem + L_MISC + 24);
            constexpr int CW = NCOPY / 16;
            __builtin_amdgcn_raw_buffer_store_b64((u32x2){tag, tag ^ KEY2 ^ far}, rs_ws,
                                                  lane < CW ? (u32)OFF_RDY + (w * CW + lane) * (NWG * 8) + g * 8 : OOB, 0, SC1);
        }
        STAMP(4);

        u32 rdy = 0, done = 0, spins = 0;
        u32x2 fl = {0, 0};
        const u32 fl_off = lane < 16 ? (u32)OFF_RDY + (g & (NCOPY - 1)) * (NWG * 8) + (w * 16 + lane) * 8 : OOB;
        const u32 cj = w * 16 * S + (lane & 15) * 4;
        u32x4 rq[4];
        u32x2 rn[4];
        u32 rd[4], rl[4];
#pragma unroll
        for (int i = 0; i < 4; ++i) {
#pragma unroll 1
            while (__builtin_popcount(rdy & ~done) < 4) {
                asm volatile("" ::: "memory");
                if (rdy | spins) __builtin_amdgcn_s_sleep(1);
                fl = __builtin_amdgcn_raw_buffer_load_b64(rs_ws, fl_off, 0, SC1);
                u32 tok = (u32)__builtin_amdgcn_ballot_w64(gr.x == tag && gr.z == (tag ^ KEY2));
                if ((~tok & 0xFFFFu) != 0) {
                    if (lane < 16 && !((tok >> lane) & 1)) gr = __builtin_amdgcn_raw_buffer_load_b128(rs_ws, tb_off, 0, SC1);
                    tok = (u32)__builtin_amdgcn_ballot_w64(gr.x == tag && gr.z == (tag ^ KEY2));
                }
                rdy = (u32)__builtin_amdgcn_ballot_w64(fl.x == tag && (fl.y ^ tag ^ KEY2) < 2u) & tok & 0xFFFFu;
                if (++spins > (1u << 16)) { gr.y = 0; rdy = 0xFFFFu; }
            }
            u32 av = rdy & ~done, pk = 0;
#pragma unroll
            for (int k = 0; k < 4; ++k) {
                pk |= (u32)__builtin_ctz(av) << (8 * k + 2);
                av &= av - 1;
            }
            done = rdy & ~av;
            const u32 sl4 = (pk >> ((lane >> 4) * 8)) & 0xFFu;
            const u32 sg = __builtin_amdgcn_ds_bpermute(sl4, gr.y);
            rl[i] = sg >> 16;
            rd[i] = (lane & 15) * 4 - (sg & 3);
            u32 off = (__umul24(sl4, S / 4) + cj + (sg & 0xFFFCu)) * 4;
            off = (int)rd[i] < (int)rl[i] ? off : OOB;
            rq[i] = __builtin_amdgcn_raw_buffer_load_b128(rs_rw, off, 0, SC1);
            rn[i] = __builtin_amdgcn_raw_buffer_load_b64(rs_ra, off >> 1, 0, SC1);
            SB();
            if (i == 0) STAMP(5);
            if (i == 3) STAMP(10);
            if (i > 0 && i < 3) sort_quad(rq[i - 1], rn[i - 1], rd[i - 1], rl[i - 1], smem, lane);
            SB();
        }
        {
            const u32 need = lane < 16 ? (gr.y >> 16) + (gr.y & 3) : 0u;
            const bool more = __builtin_amdgcn_ballot_w64(need > 64) != 0;
            stat_max(row_pkmax(gr.w), L_MISC + 16, lane == 15);
            if (need > 80 || (lane < 16 && fl.y != (tag ^ KEY2))) *(u32*)(smem + L_MISC + 4) = 1;
            if (more) {
                const u32 sg = __builtin_amdgcn_ds_bpermute(lane & 60, gr.y);
                const u32 j4 = 64 + (lane & 3) * 4, d = j4 - (sg & 3), len = sg >> 16;
                u32 off = ((w * 16 + (lane >> 2)) * S + j4 + (sg & 0xFFFCu)) * 4;
                off = d < len ? off : OOB;
                const u32x4 q = __builtin_amdgcn_raw_buffer_load_b128(rs_rw, off, 0, SC1);
                const u32x2 an = __builtin_amdgcn_raw_buffer_load_b64(rs_ra, off >> 1, 0, SC1);
                sort_quad(rq[2], rn[2], rd[2], rl[2], smem, lane);
                rq[2] = q; rn[2] = an; rd[2] = d; rl[2] = len;
            }
            sort_quad(rq[2], rn[2], rd[2], rl[2], smem, lane);
            sort_quad(rq[3], rn[3], rd[3], rl[3], smem, lane);
        }
        STAMP(6);
        asm volatile("s_waitcnt lgkmcnt(0)" ::: "memory");
        __syncthreads();
        STAMP(13);

        constexpr int NS = 12;
        u32x2 rec[NS], mg[NS];
        bool val[NS];
        const u32 cn = *(const u32*)(smem + L_BC + (w * 10 + min(lane, 9)) * 4);
        const u32x2 mi0 = *(const u32x2*)(smem + L_MISC), mi1 = *(const u32x2*)(smem + L_MISC + 16);
        const u32 l8 = lane * 8;
        auto fetch = [&](int k) {
            if (k < 10) rec[k] = *(const u32x2*)(smem + L_BIN + w * (10 * BSTR) + l8 + k * BSTR);
        };
#pragma unroll
        for (int k = 0; k < GDEPTH + 1; ++k) fetch(k);
        const u32 nsp = __builtin_amdgcn_readfirstlane(mi0.x);
        auto live = [&](int k) { return k < 10 || (u32)(((k - 10) * 16 + w) * 64) < nsp; };
        auto gather = [&](int k) {
            if (k < 10) {
                val[k] = l8 < (u32)__builtin_amdgcn_readlane(cn, k);
                mg[k] = __builtin_amdgcn_raw_buffer_load_b64(rs_m, val[k] ? (EXP == 1 ? l8 : rec[k].x >> 11) : OOB, 0, 0);
            } else if (live(k)) {
                const u32 p = ((k - 10) * 16 + w) * 64 + lane;
                val[k] = p < nsp;
                rec[k] = *(const u32x2*)(smem + L_SPILL + min(p, (u32)SPILL - 1) * 8);
                mg[k] = __builtin_amdgcn_raw_buffer_load_b64(rs_m, val[k] ? rec[k].x >> 11 : OOB, 0, 0);
            }
        };
#pragma unroll
        for (int k = 0; k < GDEPTH; ++k) gather(k);
        SB();
        if (g == 0 && t == 0) __builtin_amdgcn_raw_buffer_store_b32(tag, rs_ws, 0, 0, SC1);
        const u32 smax = __builtin_amdgcn_readfirstlane(mi1.x), smin = 0x7FFFu - __builtin_amdgcn_readfirstlane(mi1.y);
        bool slow = nsp > SPILL || __builtin_amdgcn_readfirstlane(mi0.y) != 0 || smax >= 0x7F80u || smin + (9u << 7) < smax;
        Fix F;
        f32x4 inv;
        {
            const float mmax = lo16f(smax), bound = cs * mmax;
            slow |= __builtin_amdgcn_ballot_w64(!(bound == 0.f || (bound > 0x1p-60f && bound < 0x1p60f && mmax > 0x1p-60f))) != 0;
            const int e = __builtin_amdgcn_frexp_expf(bound) - 17;
            const u32 mgl = __float_as_uint(ldexpf(12582912.0f, e)), ivl = __float_as_uint(ldexpf(1.f, e));
            float mgv[4];
#pragma unroll
            for (int k = 0; k < 4; ++k) {
                mgv[k] = __uint_as_float(__builtin_amdgcn_readlane(mgl, k));
                inv[k] = __uint_as_float(__builtin_amdgcn_readlane(ivl, k));
            }
            F.f01 = (f32x2){f0, f1};
            F.f2 = f2;
            F.mg01 = (f32x2){mgv[0], mgv[1]};
            F.mg23 = (f32x2){mgv[2], mgv[3]};
            F.nb01 = 0 - __builtin_bit_cast(u64, F.mg01);
            F.nb23 = 0 - __builtin_bit_cast(u64, F.mg23);
        }
        float ag[4];
        u32 xacc = 0;
        if (!slow) {
#pragma unroll
            for (int k = 0; k < NS; ++k) {
                SB();
                if (k + GDEPTH + 1 < NS) fetch(k + GDEPTH + 1);
                if (EXP == 2) { if (live(k)) xacc ^= mg[k].x ^ mg[k].y; }
                else if (live(k)) {
                    const f32x4 md = row_mat4(wdc, mg[k]);
                    if (val[k]) accum_fix(rec[k].x, rec[k].y, md, F, smem);
                }
                SB();
                if (k + GDEPTH < NS) gather(k + GDEPTH);
            }
            if (EXP == 2) asm volatile("" :: "v"(xacc));
            STAMP(7);
            __syncthreads();
            if (w < 10) {
                const u64 s01 = *(const u64*)(smem + L_ACC + t * 8), s23 = *(const u64*)(smem + L_ACC + ACC_STR + t * 8);
                const int q0 = (int)(u32)s01, q2 = (int)(u32)s23;
                const int q1 = (int)(u32)(s01 >> 32) - (q0 >> 31), q3 = (int)(u32)(s23 >> 32) - (q2 >> 31);
                ag[0] = (float)q0 * inv[0] * sa[0]; ag[1] = (float)q1 * inv[1] * sa[1];
                ag[2] = (float)q2 * inv[2] * sa[2]; ag[3] = (float)q3 * inv[3] * sa[3];
            }
        } else {
            __syncthreads();
            if (w < 10) {
                const EdgeC e = edge_coeffs(dv, centers, w_rbf, w_sbf);
                *(f32x4*)(smem + L_A + t * 16) = e.A0;
                *(f32x4*)(smem + L_A + A_STR + t * 16) = e.A1;
                *(f32x4*)(smem + L_A + 2 * A_STR + t * 16) = e.A2;
                gp = (u32x2){pk_bf16(e.g.x, e.g.y), pk_bf16(e.g.z, e.g.w)};
                *(u32x4*)(smem + L_BIN + t * 32) = (u32x4){0, 0, 0, 0};
                *(u32x4*)(smem + L_BIN + t * 32 + 16) = (u32x4){0, 0, 0, 0};
            }
            __syncthreads();
#pragma unroll 1
            for (int s = 0; s < 16; ++s) {
                const u32 sg = __builtin_amdgcn_readlane(gr.y, s);
                const u32 len = sg >> 16, p0 = (w * 16 + s) * S + (sg & 0xFFFFu);
#pragma unroll 1
                for (u32 q = 0; q < len; q += 64) {
                    if (q + lane < len) {
                        const u32 p = p0 + q + lane;
                        const u32 w0 = __builtin_amdgcn_raw_buffer_load_b32(rs_rw, p * 4, 0, SC1);
                        const u32 a2 = __builtin_amdgcn_raw_buffer_load_b32(rs_ra, (p >> 1) * 4, 0, SC1);
                        const u32x2 mr = __builtin_amdgcn_raw_buffer_load_b64(rs_m, w0 >> 11, 0, 0);
                        accum_slow(w0, (p & 1) ? a2 & 0xFFFF0000u : a2 << 16, mr, w_down, f0, f1, f2, smem);
                    }
                }
            }
            __syncthreads();
            if (w < 10) {
                const double* acc = (const double*)(smem + L_BIN) + t;
                ag[0] = (float)acc[0]; ag[1] = (float)acc[640]; ag[2] = (float)acc[1280]; ag[3] = (float)acc[1920];
            }
        }
        if (w < 10) {
            const f32x4 u = row_mat4(*(const u32x2*)(smem + L_WD + 32 + (lane & 3) * 8), (u32x2){pk_bf16(ag[0], ag[1]), pk_bf16(ag[2], ag[3])});
            const float mm[4] = {lo16f(mrow_.x), hi16f(mrow_.x), lo16f(mrow_.y), hi16f(mrow_.y)};
            const float gg[4] = {lo16f(gp.x), hi16f(gp.x), lo16f(gp.y), hi16f(gp.y)};
            float o[4];
#pragma unroll
            for (int k = 0; k < 4; ++k) o[k] = mm[k] + rbf16(rbf16(u[k]) * gg[k]);
            __builtin_amdgcn_raw_buffer_store_b64((u32x2){pk_bf16(o[0], o[1]), pk_bf16(o[2], o[3])}, rs_out, t * 8, 0, SC1);
        }
    }
    STAMP(9);
}
}

namespace generic {
__global__ void k_trip(const u16* m, const int* idx_in, const int* idx_out, const u16* dist,
                       const u16* angle, const u16* centers, const u16* freq,
                       const u16* w_sbf, const u16* w_down, float* acc, int T) {
    int t = blockIdx.x * blockDim.x + threadIdx.x;
    if (t >= T) return;
    int ei = idx_in[t], eo = idx_out[t];
    float a = bf2f(angle[t]);
    float d = bf2f(dist[eo]);
    float c[3];
    for (int l = 0; l < 3; ++l) c[l] = rbf16(cosf(rbf16(bf2f(freq[l]) * a)));
    float s[4] = {0, 0, 0, 0};
    for (int r = 0; r < 8; ++r) {
        float df = rbf16(d - bf2f(centers[r]));
        float rb = rbf16(expf(rbf16(-10.f * rbf16(df * df))));
        for (int l = 0; l < 3; ++l) {
            float sb = rbf16(rb * c[l]);
            for (int k = 0; k < 4; ++k) s[k] += sb * bf2f(w_sbf[(r * 3 + l) * 4 + k]);
        }
    }
    float mm[4];
    for (int k = 0; k < 4; ++k) mm[k] = bf2f(m[ei * 4 + k]);
    for (int k = 0; k < 4; ++k) {
        float md = 0;
        for (int j = 0; j < 4; ++j) md += mm[j] * bf2f(w_down[j * 4 + k]);
        float x = rbf16(rbf16(md) * rbf16(s[k]));
        atomicAdd(&acc[eo * 4 + k], x);
    }
}

__global__ void k_fin(const u16* m, const u16* dist, const u16* centers, const u16* w_rbf,
                      const u16* w_up, const float* acc, __hip_bfloat16* out, int E) {
    int e = blockIdx.x * blockDim.x + threadIdx.x;
    if (e >= E) return;
    float d = bf2f(dist[e]);
    float g[4] = {0, 0, 0, 0};
    for (int r = 0; r < 8; ++r) {
        float df = rbf16(d - bf2f(centers[r]));
        float rb = rbf16(expf(rbf16(-10.f * rbf16(df * df))));
        for (int k = 0; k < 4; ++k) g[k] += rb * bf2f(w_rbf[r * 4 + k]);
    }
    float ag[4];
    for (int k = 0; k < 4; ++k) ag[k] = rbf16(acc[e * 4 + k]);
    for (int k = 0; k < 4; ++k) {
        float u = 0;
        for (int j = 0; j < 4; ++j) u += ag[j] * bf2f(w_up[j * 4 + k]);
        float y = rbf16(rbf16(u) * rbf16(g[k]));
        out[e * 4 + k] = __float2bfloat16(bf2f(m[e * 4 + k]) + y);
    }
}

}

extern "C" void kernel_launch(void* const* d_in, const int* in_sizes, int n_in,
                              void* d_out, int out_size, void* d_ws, size_t ws_size,
                              hipStream_t stream) {
    const u16* m = (const u16*)d_in[0];
    const u32* idx_in = (const u32*)d_in[1];
    const u32* idx_out = (const u32*)d_in[2];
    const u16* dist = (const u16*)d_in[3];
    const u16* angle = (const u16*)d_in[4];
    const u16* centers = (const u16*)d_in[5];
    const u16* freq = (const u16*)d_in[6];
    const u16* w_rbf = (const u16*)d_in[7];
    const u16* w_sbf = (const u16*)d_in[8];
    const u16* w_down = (const u16*)d_in[9];
    const u16* w_up = (const u16*)d_in[10];
    const int T = in_sizes[1], E = in_sizes[3];
    if (T != fast::kT || E != fast::kE || ws_size < fast::WS_NEED) {
        float* acc = (float*)d_ws;
        hipMemsetAsync(acc, 0, (size_t)E * 4 * sizeof(float), stream);
        generic::k_trip<<<(T + 255) / 256, 256, 0, stream>>>(m, (const int*)idx_in, (const int*)idx_out, dist, angle, centers, freq, w_sbf, w_down, acc, T);
        generic::k_fin<<<(E + 255) / 256, 256, 0, stream>>>(m, dist, centers, w_rbf, w_up, acc, (__hip_bfloat16*)d_out, E);
        return;
    }
    fast::k_main<<<fast::NWG, 1024, 0, stream>>>(m, idx_in, idx_out, dist, angle, centers, freq, w_rbf, w_sbf, w_down, w_up, (u16*)d_out, (char*)d_ws);
}
```
